```python
import torch

def _softmax(x, axis=-1):
    return torch.softmax(x, dim=axis)

def _gelu(x):
    return torch.nn.functional.gelu(x, approximate='tanh')

def _silu(x):
    return torch.nn.functional.silu(x)

def _sigmoid(x):
    return torch.sigmoid(x)

def _layernorm(x, axis=-1, eps=1e-05):
    m = x.mean(dim=axis, keepdim=True)
    v = x.var(dim=axis, keepdim=True, unbiased=False)
    return (x - m) / torch.sqrt(v + eps)

def _scatter_add(tgt, idx, src, axis=0):
    return tgt.clone().index_add_(axis, idx, src)

def _segment_sum(src, seg, n):
    out = torch.zeros((n,) + src.shape[1:], dtype=src.dtype)
    return out.index_add_(0, seg, src)

def _pad(x, pw):
    flat = tuple((v for pair in reversed(pw) for v in pair))
    return torch.nn.functional.pad(x, flat)
kernel_name = 'standalone__autoregressive_conv_wavenet__speech'

def setup_inputs():

    _w = lambda a: (lambda t: t.to(torch.bfloat16) if t.is_floating_point() else t)(torch.as_tensor(a))
    B, T, D, K = (2, 16000, 144, 5)
    return {'x': _w((lambda *s: torch.randn(*s, dtype=torch.bfloat16) * 0.1)(B, T, D)), 'w': _w((lambda *s: torch.randn(*s, dtype=torch.bfloat16) * 0.1)(K, D, 2 * D)), 'w_o': _w((lambda *s: torch.randn(*s, dtype=torch.bfloat16) * 0.05)(D, D))}

def reference(x, w, w_o):
    B, T, D = x.shape
    K = w.shape[0]
    dil = 2
    pad = (K - 1) * dil
    xp = _pad(x, ((0, 0), (pad, 0), (0, 0)))
    h = torch.einsum('btc,cd->btd', xp[:, pad:pad + T, :], w[0])
    for i in range(1, K):
        off = pad - i * dil
        h = h + torch.einsum('btc,cd->btd', xp[:, off:off + T, :], w[i])
    f = h[..., :D].clone()
    g = h[..., D:].clone()
    z = torch.tanh(f) * _sigmoid(g)
    return z @ w_o
```

```cpp
#include <hip/hip_runtime.h>
#include <hip/hip_bf16.h>
typedef unsigned short u16;
__device__ __forceinline__ float b2f(u16 v){ return __uint_as_float((unsigned)v<<16); }
__device__ __forceinline__ u16 f2b(float f){ unsigned u=__float_as_uint(f); u+=0x7fff+((u>>16)&1); return (u16)(u>>16); }
constexpr int T=16000,D=144,K=5;
constexpr int R=16;
__global__ __launch_bounds__(288) void naive_k(const u16* x,const u16* w,const u16* wo,u16* out){
  __shared__ float xs[(R+8)*D];
  __shared__ float hs[R*2*D];
  int blk=blockIdx.x; int b=blk/(T/R); int t0=(blk%(T/R))*R;
  for(int i=threadIdx.x;i<(R+8)*D;i+=288){ int r=i/D,c=i%D; int t=t0-8+r; xs[i]= t>=0? b2f(x[((size_t)b*T+t)*D+c]):0.f; }
  __syncthreads();
  int n=threadIdx.x;
  float acc[R]; for(int r=0;r<R;++r)acc[r]=0;
  for(int i=0;i<K;++i) for(int c=0;c<D;++c){ float wv=b2f(w[(i*D+c)*2*D+n]);
    #pragma unroll
    for(int r=0;r<R;++r) acc[r]+=xs[(r+8-2*i)*D+c]*wv; }
  for(int r=0;r<R;++r) hs[r*2*D+n]=acc[r];
  __syncthreads();
  for(int i=threadIdx.x;i<R*D;i+=288){ int r=i/D,c=i%D; float f=hs[r*2*D+c],g=hs[r*2*D+D+c]; float z=tanhf(f)/(1.f+expf(-g)); xs[i]=b2f(f2b(z)); }
  __syncthreads();
  if(n<D){ float o[R]; for(int r=0;r<R;++r)o[r]=0;
    for(int c=0;c<D;++c){ float wv=b2f(wo[c*D+n]);
      #pragma unroll
      for(int r=0;r<R;++r) o[r]+=xs[r*D+c]*wv; }
    for(int r=0;r<R;++r) out[((size_t)b*T+t0+r)*D+n]=f2b(o[r]); }
}
extern "C" void kernel_launch(void* const* d_in, const int* in_sizes, int n_in, void* d_out, int out_size, void* d_ws, size_t ws_size, hipStream_t stream) {
  naive_k<<<2*T/R,288,0,stream>>>((const u16*)d_in[0],(const u16*)d_in[1],(const u16*)d_in[2],(u16*)d_out);
}
```

```cpp
#include <hip/hip_runtime.h>
#include <hip/hip_bf16.h>

__global__ __launch_bounds__(256, 1) void standalone__autoregressive_conv_wavenet__speech_kernel(const void* x, const void* w, const void* wo, void* out) {
  __shared__ __attribute__((aligned(1024))) char lds[163840];
  unsigned tid = threadIdx.x;
  unsigned bid = blockIdx.x;
  unsigned ldsb = (unsigned)(size_t)lds;
  auto karg = __builtin_amdgcn_kernarg_segment_ptr();

  asm volatile("v_mov_b32 v255, %[tid]\n\ts_load_dwordx8 s[8:15], %[karg], 0x0\n\tv_and_b32 v244, 63, v255\n\tv_lshrrev_b32 v245, 6, v255\n\ts_nop 0\n\tv_readfirstlane_b32 s36, v245\n\ts_cmp_ge_u32 %[bid], 125\n\ts_cselect_b32 s41, 125, 0\n\ts_sub_u32 s37, %[bid], s41\n\ts_mul_i32 s37, s37, 36864\n\ts_cmp_ge_u32 %[bid], 125\n\ts_cselect_b32 s49, 4608000, 0\n\ts_lshl_b32 s47, s36, 10\n\ts_mul_i32 s39, s36, 9216\n\ts_mov_b32 s38, s39\n\ts_add_u32 s40, s47, 110592\n\ts_add_u32 s41, s37, s47\n\ts_sub_u32 s41, s41, 2304\n\tv_lshlrev_b32 v213, 4, v244\n\tv_add_u32 v211, s41, v213\n\tv_lshrrev_b32 v245, 2, v244\n\tv_mul_u32_u24 v245, 576, v245\n\tv_lshrrev_b32 v246, 5, v244\n\tv_lshlrev_b32 v246, 1, v246\n\tv_and_b32 v247, 3, v244\n\tv_xor_b32 v246, v246, v247\n\tv_lshl_add_u32 v202, v246, 4, v245\n\ts_getpc_b64 s[60:61]\n\tCS%=:\n\ts_and_b32 s61, s61, 0xffff\n\ts_mov_b32 s62, CE%=-CS%=\n\ts_mov_b32 s63, 0x20000\n\ts_lshr_b32 s64, %[bid], 3\n\ts_lshl_b32 s65, s36, 5\n\ts_add_u32 s65, s65, s64\n\ts_lshl_b32 s65, s65, 10\n\tbuffer_load_dwordx4 v[236:239], v213, s[60:63], s65 offen\n\ts_mov_b32 s42, 0x4038aa3b\n\ts_mov_b32 s43, 0xbfb8aa3b\n\ts_mov_b32 s44, 0x7fffffff\n\ts_cmp_lt_u32 s36, 2\n\ts_cselect_b32 s45, 0, 0x40000000\n\tv_and_b32 v245, 15, v244\n\tv_lshrrev_b32 v246, 4, v244\n\tv_mul_u32_u24 v247, 288, v245\n\tv_lshl_add_u32 v219, v246, 4, v247\n\tv_add_u32 v192, 110592, v219\n\ts_mul_i32 s46, s36, 4608\n\tv_add_u32 v193, s46, v192\n\tv_lshl_add_u32 v220, v246, 3, v247\n\tv_add_u32 v205, 110592, v220\n\tv_add_u32 v206, s46, v205\n\tv_lshrrev_b32 v248, 5, v244\n\tv_mul_u32_u24 v249, 864, v248\n\tv_sub_u32 v203, v192, v249\n\tv_sub_u32 v204, v193, v249\n\ts_lshl_b32 s48, s36, 6\n\tv_add_u32 v219, s48, v219\n\tv_and_b32 v245, 12, v244\n\tv_lshlrev_b32 v245, 4, v245\n\tv_and_b32 v247, 3, v244\n\tv_lshl_add_u32 v245, v247, 3, v245\n\tv_and_b32 v249, 1, v246\n\tv_mul_u32_u24 v247, 9216, v248\n\tv_add_u32 v247, v247, v245\n\tv_mul_u32_u24 v196, 544, v249\n\tv_add_u32 v196, v196, v247\n\tv_mul_u32_u24 v197, 480, v249\n\tv_add3_u32 v197, v197, v247, 32\n\tv_add_u32 v194, s47, v196\n\tv_add_u32 v195, s47, v197\n\tv_add_u32 v198, 73728, v194\n\tv_add_u32 v199, 73728, v195\n\tv_add_u32 v200, 73728, v196\n\tv_add_u32 v201, 73728, v197\n\tv_lshl_add_u32 v247, v246, 8, v245\n\tv_lshl_add_u32 v209, v248, 5, v247\n\tv_add_u32 v209, 73728, v209\n\tv_lshlrev_b32 v249, 5, v248\n\tv_sub_u32 v210, v247, v249\n\tv_add_u32 v210, 73760, v210\n\tv_add_u32 v207, s47, v209\n\tv_add_u32 v208, s47, v210\n\tv_lshrrev_b32 v245, 2, v244\n\tv_mul_u32_u24 v245, 288, v245\n\tv_and_b32 v247, 3, v244\n\tv_lshl_add_u32 v217, v247, 4, v245\n\tv_add_u32 v217, s48, v217\n\tv_lshl_add_u32 v218, v247, 3, v245\n\tv_add_u32 v214, s47, v213\n\tv_lshlrev_b32 v215, 3, v244\n\tv_add_u32 v216, s46, v215\n\ts_mul_i32 s48, s36, 1152\n\tv_add_u32 v227, s48, v213\n\tv_add_u32 v229, s37, v227\n\tv_cmp_gt_u32 vcc, 8, v244\n\tv_bfrev_b32 v245, 1\n\ts_nop 0\n\tv_cndmask_b32 v230, v245, v229, vcc\n\ts_mul_i32 s48, s36, 2304\n\tv_add_u32 v231, s48, v213\n\tv_add_u32 v233, s37, v231\n\tv_cmp_gt_u32 vcc, 16, v244\n\tv_bfrev_b32 v245, 1\n\ts_nop 0\n\tv_cndmask_b32 v234, v245, v233, vcc\n\tv_add_u32 v221, 0x10000, v213\n\tv_add_u32 v222, 0x10000, v214\n\tv_add_u32 v223, 0x10000, v215\n\tv_add_u32 v224, 0x10000, v216\n\tv_add_u32 v225, 0x10000, v219\n\tv_add_u32 v226, 0x10000, v220\n\tv_add_u32 v228, 0x10000, v227\n\tv_add_u32 v232, 0x10000, v231\n\ts_waitcnt lgkmcnt(0)\n\ts_add_u32 s16, s8, s49\n\ts_addc_u32 s17, s9, 0\n\ts_and_b32 s17, s17, 0xffff\n\ts_mov_b32 s18, 4608000\n\ts_mov_b32 s19, 0x20000\n\ts_add_u32 m0, s40, 0\n\ts_nop 0\n\tbuffer_load_dwordx4 v211, s[16:19], 0 offen nt lds\n\tv_add_u32 v212, 0x1000, v211\n\ts_add_u32 m0, s40, 4096\n\ts_nop 0\n\tbuffer_load_dwordx4 v212, s[16:19], 0 offen nt lds\n\tv_add_u32 v211, 0x1000, v212\n\ts_add_u32 m0, s40, 8192\n\ts_nop 0\n\tbuffer_load_dwordx4 v211, s[16:19], 0 offen nt lds\n\tv_add_u32 v212, 0x1000, v211\n\ts_add_u32 m0, s40, 12288\n\ts_nop 0\n\tbuffer_load_dwordx4 v212, s[16:19], 0 offen nt lds\n\tv_add_u32 v211, 0x1000, v212\n\ts_add_u32 m0, s40, 16384\n\ts_nop 0\n\tbuffer_load_dwordx4 v211, s[16:19], 0 offen nt lds\n\tv_add_u32 v212, 0x1000, v211\n\ts_add_u32 m0, s40, 20480\n\ts_nop 0\n\tbuffer_load_dwordx4 v212, s[16:19], 0 offen nt lds\n\tv_add_u32 v211, 0x1000, v212\n\ts_add_u32 m0, s40, 24576\n\ts_nop 0\n\tbuffer_load_dwordx4 v211, s[16:19], 0 offen nt lds\n\tv_add_u32 v212, 0x1000, v211\n\ts_add_u32 m0, s40, 28672\n\ts_nop 0\n\tbuffer_load_dwordx4 v212, s[16:19], 0 offen nt lds\n\ts_mov_b32 s20, s10\n\ts_and_b32 s21, s11, 0xffff\n\ts_mov_b32 s22, 414720\n\ts_mov_b32 s23, 0x20000\n\ts_mov_b32 s24, s12\n\ts_and_b32 s25, s13, 0xffff\n\ts_mov_b32 s26, 41472\n\ts_mov_b32 s27, 0x20000\n\ts_mul_i32 s65, s64, 4096\n\ts_mul_i32 s66, s36, 1024\n\ts_add_u32 s65, s65, s66\n\ts_min_u32 s65, s65, 413696\n\tbuffer_load_dwordx4 v[236:239], v213, s[20:23], s65 offen offset:0\n\ts_add_u32 m0, s39, 0\n\ts_nop 0\n\tbuffer_load_dwordx4 v202, s[20:23], s38 offen offset:0 lds\n\ts_add_u32 m0, s39, 960\n\ts_nop 0\n\tbuffer_load_dwordx4 v202, s[20:23], s38 offen offset:64 lds\n\ts_add_u32 m0, s39, 1920\n\ts_nop 0\n\tbuffer_load_dwordx4 v202, s[20:23], s38 offen offset:128 lds\n\ts_add_u32 m0, s39, 2880\n\ts_nop 0\n\tbuffer_load_dwordx4 v202, s[20:23], s38 offen offset:192 lds\n\ts_add_u32 m0, s39, 3840\n\ts_nop 0\n\tbuffer_load_dwordx4 v202, s[20:23], s38 offen offset:256 lds\n\ts_add_u32 m0, s39, 4800\n\ts_nop 0\n\tbuffer_load_dwordx4 v202, s[20:23], s38 offen offset:320 lds\n\ts_add_u32 m0, s39, 5760\n\ts_nop 0\n\tbuffer_load_dwordx4 v202, s[20:23], s38 offen offset:384 lds\n\ts_add_u32 m0, s39, 6720\n\ts_nop 0\n\tbuffer_load_dwordx4 v202, s[20:23], s38 offen offset:448 lds\n\ts_add_u32 m0, s39, 7680\n\ts_nop 0\n\tbuffer_load_dwordx4 v202, s[20:23], s38 offen offset:512 lds\n\ts_add_u32 s38, s38, 36864\n\ts_add_u32 s28, s14, s49\n\ts_addc_u32 s29, s15, 0\n\ts_and_b32 s29, s29, 0xffff\n\ts_mov_b32 s30, 4608000\n\ts_mov_b32 s31, 0x20000\n\ts_add_u32 m0, s39, 36864\n\ts_nop 0\n\tbuffer_load_dwordx4 v202, s[20:23], s38 offen offset:0 lds\n\ts_add_u32 m0, s39, 37824\n\ts_nop 0\n\tbuffer_load_dwordx4 v202, s[20:23], s38 offen offset:64 lds\n\ts_add_u32 m0, s39, 38784\n\ts_nop 0\n\tbuffer_load_dwordx4 v202, s[20:23], s38 offen offset:128 lds\n\ts_add_u32 m0, s39, 39744\n\ts_nop 0\n\tbuffer_load_dwordx4 v202, s[20:23], s38 offen offset:192 lds\n\ts_add_u32 m0, s39, 40704\n\ts_nop 0\n\tbuffer_load_dwordx4 v202, s[20:23], s38 offen offset:256 lds\n\ts_add_u32 m0, s39, 41664\n\ts_nop 0\n\tbuffer_load_dwordx4 v202, s[20:23], s38 offen offset:320 lds\n\ts_add_u32 m0, s39, 42624\n\ts_nop 0\n\tbuffer_load_dwordx4 v202, s[20:23], s38 offen offset:384 lds\n\ts_add_u32 m0, s39, 43584\n\ts_nop 0\n\tbuffer_load_dwordx4 v202, s[20:23], s38 offen offset:448 lds\n\ts_add_u32 m0, s39, 44544\n\ts_nop 0\n\tbuffer_load_dwordx4 v202, s[20:23], s38 offen offset:512 lds\n\ts_add_u32 s38, s38, 36864\n\ts_waitcnt vmcnt(9)\n\ts_barrier\n\ts_add_u32 m0, s39, 73728\n\ts_mov_b32 s41, 0\n\tds_read_b64_tr_b16 a[0:1], v194 offset:0\n\tv_add_u32 v211, 0x1000, v212\n\tv_add_u32 v212, 0x1000, v211\n\tds_read_b64_tr_b16 a[2:3], v194 offset:256\n\tv_min_u32 v212, 4607984, v212\n\tds_read_b64_tr_b16 a[4:5], v195 offset:4096\n\tds_read_b64_tr_b16 a[6:7], v195 offset:4352\n\tds_read_b64_tr_b16 a[8:9], v195 offset:0\n\tds_read_b64_tr_b16 a[10:11], v195 offset:256\n\tds_read_b64_tr_b16 a[12:13], v194 offset:5120\n\tds_read_b64_tr_b16 a[14:15], v194 offset:5376\n\tds_read_b64_tr_b16 a[16:17], v196 offset:4096\n\tds_read_b64_tr_b16 a[18:19], v196 offset:4352\n\tds_read_b64_tr_b16 a[20:21], v197 offset:8192\n\tds_read_b64_tr_b16 a[22:23], v197 offset:8448\n\tds_read_b128 a[248:251], v193 offset:2304\n\tds_read_b128 a[216:219], v192 offset:2304\n\tds_read_b128 a[220:223], v192 offset:6912\n\tds_read_b128 a[224:227], v192 offset:11520\n\tds_read_b128 a[228:231], v192 offset:16128\n\tds_read_b128 a[232:235], v192 offset:20736\n\ts_waitcnt lgkmcnt(5)\n\tv_mfma_f32_16x16x32_bf16 v[128:131], a[16:19], a[248:251], 0\n\tbuffer_load_dwordx4 v202, s[20:23], s38 offen offset:0 lds\n\tv_mfma_f32_16x16x32_bf16 v[132:135], a[20:23], a[248:251], 0\n\ts_waitcnt lgkmcnt(3)\n\tds_read_b128 a[236:239], v192 offset:25344\n\tv_mfma_f32_16x16x32_bf16 v[0:3], a[0:3], a[216:219], 0\n\tds_read_b64_tr_b16 a[24:25], v194 offset:18432\n\tv_mfma_f32_16x16x32_bf16 v[4:7], a[4:7], a[216:219], 0\n\ts_add_u32 m0, s39, 74688\n\tds_read_b64_tr_b16 a[26:27], v194 offset:18688\n\tv_mfma_f32_16x16x32_bf16 v[8:11], a[8:11], a[216:219], 0\n\tbuffer_load_dwordx4 v202, s[20:23], s38 offen offset:64 lds\n\tv_mfma_f32_16x16x32_bf16 v[12:15], a[12:15], a[216:219], 0\n\tds_read_b64_tr_b16 a[28:29], v195 offset:22528\n\tv_mfma_f32_16x16x32_bf16 v[28:31], a[12:15], a[220:223], 0\n\tds_read_b64_tr_b16 a[30:31], v195 offset:22784\n\tv_mfma_f32_16x16x32_bf16 v[24:27], a[8:11], a[220:223], 0\n\ts_add_u32 m0, s39, 75648\n\tds_read_b64_tr_b16 a[32:33], v195 offset:18432\n\tv_mfma_f32_16x16x32_bf16 v[20:23], a[4:7], a[220:223], 0\n\tbuffer_load_dwordx4 v202, s[20:23], s38 offen offset:128 lds\n\tv_mfma_f32_16x16x32_bf16 v[16:19], a[0:3], a[220:223], 0\n\ts_waitcnt lgkmcnt(3)\n\tds_read_b64_tr_b16 a[34:35], v195 offset:18688\n\tv_mfma_f32_16x16x32_bf16 v[32:35], a[0:3], a[224:227], 0\n\tds_read_b64_tr_b16 a[36:37], v194 offset:23552\n\tv_mfma_f32_16x16x32_bf16 v[36:39], a[4:7], a[224:227], 0\n\ts_add_u32 m0, s39, 76608\n\tds_read_b64_tr_b16 a[38:39], v194 offset:23808\n\tv_mfma_f32_16x16x32_bf16 v[40:43], a[8:11], a[224:227], 0\n\tbuffer_load_dwordx4 v202, s[20:23], s38 offen offset:192 lds\n\tv_mfma_f32_16x16x32_bf16 v[44:47], a[12:15], a[224:227], 0\n\tds_read_b64_tr_b16 a[40:41], v196 offset:22528\n\tv_mfma_f32_16x16x32_bf16 v[60:63], a[12:15], a[228:231], 0\n\tds_read_b64_tr_b16 a[42:43], v196 offset:22784\n\tv_mfma_f32_16x16x32_bf16 v[56:59], a[8:11], a[228:231], 0\n\ts_add_u32 m0, s39, 77568\n\tds_read_b64_tr_b16 a[44:45], v197 offset:26624\n\tv_mfma_f32_16x16x32_bf16 v[52:55], a[4:7], a[228:231], 0\n\tbuffer_load_dwordx4 v202, s[20:23], s38 offen offset:256 lds\n\tv_mfma_f32_16x16x32_bf16 v[48:51], a[0:3], a[228:231], 0\n\tds_read_b64_tr_b16 a[46:47], v197 offset:26880\n\tv_mfma_f32_16x16x32_bf16 v[64:67], a[0:3], a[232:235], 0\n\tds_read_b128 a[248:251], v193 offset:2368\n\tv_mfma_f32_16x16x32_bf16 v[68:71], a[4:7], a[232:235], 0\n\ts_add_u32 m0, s39, 78528\n\tds_read_b128 a[216:219], v192 offset:2368\n\tv_mfma_f32_16x16x32_bf16 v[72:75], a[8:11], a[232:235], 0\n\tbuffer_load_dwordx4 v202, s[20:23], s38 offen offset:320 lds\n\tv_mfma_f32_16x16x32_bf16 v[76:79], a[12:15], a[232:235], 0\n\tds_read_b128 a[220:223], v192 offset:6976\n\tv_mfma_f32_16x16x32_bf16 v[92:95], a[12:15], a[236:239], 0\n\tds_read_b128 a[224:227], v192 offset:11584\n\tv_mfma_f32_16x16x32_bf16 v[88:91], a[8:11], a[236:239], 0\n\ts_add_u32 m0, s39, 79488\n\tds_read_b128 a[228:231], v192 offset:16192\n\tv_mfma_f32_16x16x32_bf16 v[84:87], a[4:7], a[236:239], 0\n\tbuffer_load_dwordx4 v202, s[20:23], s38 offen offset:384 lds\n\tv_mfma_f32_16x16x32_bf16 v[80:83], a[0:3], a[236:239], 0\n\ts_waitcnt vmcnt(7) lgkmcnt(3)\n\ts_barrier\n\tds_read_b128 a[232:235], v192 offset:20800\n\tv_mfma_f32_16x16x32_bf16 v[128:131], a[40:43], a[248:251], v[128:131]\n\tds_read_b128 a[236:239], v192 offset:25408\n\tv_mfma_f32_16x16x32_bf16 v[132:135], a[44:47], a[248:251], v[132:135]\n\ts_add_u32 m0, s39, 80448\n\tds_read_b64_tr_b16 a[48:49], v194 offset:36864\n\tv_mfma_f32_16x16x32_bf16 v[0:3], a[24:27], a[216:219], v[0:3]\n\tbuffer_load_dwordx4 v202, s[20:23], s38 offen offset:448 lds\n\tv_mfma_f32_16x16x32_bf16 v[4:7], a[28:31], a[216:219], v[4:7]\n\tds_read_b64_tr_b16 a[50:51], v194 offset:37120\n\tv_mfma_f32_16x16x32_bf16 v[8:11], a[32:35], a[216:219], v[8:11]\n\tds_read_b64_tr_b16 a[52:53], v195 offset:40960\n\tv_mfma_f32_16x16x32_bf16 v[12:15], a[36:39], a[216:219], v[12:15]\n\ts_waitcnt lgkmcnt(4)\n\ts_add_u32 m0, s39, 81408\n\tds_read_b64_tr_b16 a[54:55], v195 offset:41216\n\tv_mfma_f32_16x16x32_bf16 v[28:31], a[36:39], a[220:223], v[28:31]\n\tbuffer_load_dwordx4 v202, s[20:23], s38 offen offset:512 lds\n\ts_add_u32 s38, s38, 36864\n\tv_mfma_f32_16x16x32_bf16 v[24:27], a[32:35], a[220:223], v[24:27]\n\tds_read_b64_tr_b16 a[56:57], v195 offset:36864\n\tv_mfma_f32_16x16x32_bf16 v[20:23], a[28:31], a[220:223], v[20:23]\n\tds_read_b64_tr_b16 a[58:59], v195 offset:37120\n\tv_mfma_f32_16x16x32_bf16 v[16:19], a[24:27], a[220:223], v[16:19]\n\ts_add_u32 m0, s40, 32768\n\tds_read_b64_tr_b16 a[60:61], v194 offset:41984\n\tv_mfma_f32_16x16x32_bf16 v[32:35], a[24:27], a[224:227], v[32:35]\n\tbuffer_load_dwordx4 v211, s[16:19], 0 offen nt lds\n\tv_mfma_f32_16x16x32_bf16 v[36:39], a[28:31], a[224:227], v[36:39]\n\tds_read_b64_tr_b16 a[62:63], v194 offset:42240\n\tv_add_u32 v211, s46, v213\n\tv_mfma_f32_16x16x32_bf16 v[40:43], a[32:35], a[224:227], v[40:43]\n\tds_read_b64_tr_b16 a[64:65], v196 offset:40960\n\tv_mfma_f32_16x16x32_bf16 v[44:47], a[36:39], a[224:227], v[44:47]\n\ts_add_u32 m0, s40, 36864\n\tds_read_b64_tr_b16 a[66:67], v196 offset:41216\n\tv_mfma_f32_16x16x32_bf16 v[60:63], a[36:39], a[228:231], v[60:63]\n\tbuffer_load_dwordx4 v212, s[16:19], 0 offen nt lds\n\tv_mfma_f32_16x16x32_bf16 v[56:59], a[32:35], a[228:231], v[56:59]\n\tds_read_b64_tr_b16 a[68:69], v197 offset:45056\n\tv_add_u32 v212, s46, v226\n\tv_mfma_f32_16x16x32_bf16 v[52:55], a[28:31], a[228:231], v[52:55]\n\tds_read_b64_tr_b16 a[70:71], v197 offset:45312\n\tv_mfma_f32_16x16x32_bf16 v[48:51], a[24:27], a[228:231], v[48:51]\n\ts_add_u32 m0, s39, 0\n\tds_read_b128 a[248:251], v193 offset:2432\n\tv_mfma_f32_16x16x32_bf16 v[64:67], a[24:27], a[232:235], v[64:67]\n\tbuffer_load_dwordx4 v202, s[20:23], s38 offen offset:0 lds\n\tv_mfma_f32_16x16x32_bf16 v[68:71], a[28:31], a[232:235], v[68:71]\n\tds_read_b128 a[216:219], v192 offset:2432\n\tv_mfma_f32_16x16x32_bf16 v[72:75], a[32:35], a[232:235], v[72:75]\n\tds_read_b128 a[220:223], v192 offset:7040\n\tv_mfma_f32_16x16x32_bf16 v[76:79], a[36:39], a[232:235], v[76:79]\n\ts_waitcnt lgkmcnt(4)\n\ts_add_u32 m0, s39, 960\n\tds_read_b128 a[224:227], v192 offset:11648\n\tv_mfma_f32_16x16x32_bf16 v[92:95], a[36:39], a[236:239], v[92:95]\n\tbuffer_load_dwordx4 v202, s[20:23], s38 offen offset:64 lds\n\tv_mfma_f32_16x16x32_bf16 v[88:91], a[32:35], a[236:239], v[88:91]\n\tds_read_b128 a[228:231], v192 offset:16256\n\tv_mfma_f32_16x16x32_bf16 v[84:87], a[28:31], a[236:239], v[84:87]\n\tds_read_b128 a[232:235], v192 offset:20864\n\tv_mfma_f32_16x16x32_bf16 v[80:83], a[24:27], a[236:239], v[80:83]\n\ts_waitcnt lgkmcnt(4)\n\ts_add_u32 m0, s39, 1920\n\tds_read_b128 a[236:239], v192 offset:25472\n\tv_mfma_f32_16x16x32_bf16 v[128:131], a[64:67], a[248:251], v[128:131]\n\tbuffer_load_dwordx4 v202, s[20:23], s38 offen offset:128 lds\n\tv_mfma_f32_16x16x32_bf16 v[132:135], a[68:71], a[248:251], v[132:135]\n\tds_read_b64_tr_b16 a[72:73], v194 offset:55296\n\tv_mfma_f32_16x16x32_bf16 v[0:3], a[48:51], a[216:219], v[0:3]\n\tds_read_b64_tr_b16 a[74:75], v194 offset:55552\n\tv_mfma_f32_16x16x32_bf16 v[4:7], a[52:55], a[216:219], v[4:7]\n\ts_add_u32 m0, s39, 2880\n\tds_read_b64_tr_b16 a[76:77], v195 offset:59392\n\tv_mfma_f32_16x16x32_bf16 v[8:11], a[56:59], a[216:219], v[8:11]\n\tbuffer_load_dwordx4 v202, s[20:23], s38 offen offset:192 lds\n\tv_mfma_f32_16x16x32_bf16 v[12:15], a[60:63], a[216:219], v[12:15]\n\ts_waitcnt lgkmcnt(3)\n\tds_read_b64_tr_b16 a[78:79], v195 offset:59648\n\tv_mfma_f32_16x16x32_bf16 v[28:31], a[60:63], a[220:223], v[28:31]\n\tds_read_b64_tr_b16 a[80:81], v195 offset:55296\n\tv_mfma_f32_16x16x32_bf16 v[24:27], a[56:59], a[220:223], v[24:27]\n\ts_add_u32 m0, s39, 3840\n\tds_read_b64_tr_b16 a[82:83], v195 offset:55552\n\tv_mfma_f32_16x16x32_bf16 v[20:23], a[52:55], a[220:223], v[20:23]\n\tbuffer_load_dwordx4 v202, s[20:23], s38 offen offset:256 lds\n\tv_mfma_f32_16x16x32_bf16 v[16:19], a[48:51], a[220:223], v[16:19]\n\tds_read_b64_tr_b16 a[84:85], v194 offset:60416\n\tv_mfma_f32_16x16x32_bf16 v[32:35], a[48:51], a[224:227], v[32:35]\n\tds_read_b64_tr_b16 a[86:87], v194 offset:60672\n\tv_mfma_f32_16x16x32_bf16 v[36:39], a[52:55], a[224:227], v[36:39]\n\ts_add_u32 m0, s39, 4800\n\tds_read_b64_tr_b16 a[88:89], v196 offset:59392\n\tv_mfma_f32_16x16x32_bf16 v[40:43], a[56:59], a[224:227], v[40:43]\n\tbuffer_load_dwordx4 v202, s[20:23], s38 offen offset:320 lds\n\tv_mfma_f32_16x16x32_bf16 v[44:47], a[60:63], a[224:227], v[44:47]\n\tds_read_b64_tr_b16 a[90:91], v196 offset:59648\n\tv_mfma_f32_16x16x32_bf16 v[60:63], a[60:63], a[228:231], v[60:63]\n\tds_read_b64_tr_b16 a[92:93], v197 offset:63488\n\tv_mfma_f32_16x16x32_bf16 v[56:59], a[56:59], a[228:231], v[56:59]\n\ts_add_u32 m0, s39, 5760\n\tds_read_b64_tr_b16 a[94:95], v197 offset:63744\n\tv_mfma_f32_16x16x32_bf16 v[52:55], a[52:55], a[228:231], v[52:55]\n\tbuffer_load_dwordx4 v202, s[20:23], s38 offen offset:384 lds\n\tv_mfma_f32_16x16x32_bf16 v[48:51], a[48:51], a[228:231], v[48:51]\n\tds_read_b128 a[248:251], v193 offset:2496\n\tv_mfma_f32_16x16x32_bf16 v[64:67], a[48:51], a[232:235], v[64:67]\n\tds_read_b128 a[216:219], v192 offset:2496\n\tv_mfma_f32_16x16x32_bf16 v[68:71], a[52:55], a[232:235], v[68:71]\n\ts_add_u32 m0, s39, 6720\n\tds_read_b128 a[220:223], v192 offset:7104\n\tv_mfma_f32_16x16x32_bf16 v[72:75], a[56:59], a[232:235], v[72:75]\n\tbuffer_load_dwordx4 v202, s[20:23], s38 offen offset:448 lds\n\tv_mfma_f32_16x16x32_bf16 v[76:79], a[60:63], a[232:235], v[76:79]\n\tds_read_b128 a[224:227], v192 offset:11712\n\tv_mfma_f32_16x16x32_bf16 v[92:95], a[60:63], a[236:239], v[92:95]\n\tds_read_b128 a[228:231], v192 offset:16320\n\tv_mfma_f32_16x16x32_bf16 v[88:91], a[56:59], a[236:239], v[88:91]\n\ts_add_u32 m0, s39, 7680\n\tds_read_b128 a[232:235], v192 offset:20928\n\tv_mfma_f32_16x16x32_bf16 v[84:87], a[52:55], a[236:239], v[84:87]\n\tbuffer_load_dwordx4 v202, s[20:23], s38 offen offset:512 lds\n\ts_add_u32 s38, s38, 36864\n\tv_mfma_f32_16x16x32_bf16 v[80:83], a[48:51], a[236:239], v[80:83]\n\ts_mul_i32 s65, s64, 12288\n\ts_mul_i32 s66, s36, 3072\n\ts_add_u32 s65, s65, s66\n\ts_waitcnt lgkmcnt(3)\n\tv_mfma_f32_16x16x32_bf16 v[128:131], a[88:91], a[248:251], v[128:131]\n\ts_add_u32 s65, s65, 126976\n\ts_min_u32 s65, s65, 411648\n\tds_read_b128 a[236:239], v192 offset:25536\n\tv_mfma_f32_16x16x32_bf16 v[132:135], a[92:95], a[248:251], v[132:135]\n\tbuffer_load_dwordx4 v[236:239], v213, s[20:23], s65 offen offset:0\n\tv_mfma_f32_16x16x32_bf16 v[0:3], a[72:75], a[216:219], v[0:3]\n\tds_read_b128 a[248:251], v204 offset:2560\n\tbuffer_load_dwordx4 v[236:239], v213, s[20:23], s65 offen offset:1024\n\tv_mfma_f32_16x16x32_bf16 v[4:7], a[76:79], a[216:219], v[4:7]\n\tbuffer_load_dwordx4 v[236:239], v213, s[20:23], s65 offen offset:2048\n\ts_lshl_b32 s65, s36, 5\n\tv_mfma_f32_16x16x32_bf16 v[8:11], a[80:83], a[216:219], v[8:11]\n\ts_add_u32 s65, s65, s64\n\ts_lshl_b32 s65, s65, 10\n\ts_min_u32 s65, s65, 40448\n\tv_mfma_f32_16x16x32_bf16 v[12:15], a[84:87], a[216:219], v[12:15]\n\ts_add_u32 m0, s39, 36864\n\tds_read_b128 a[216:219], v203 offset:2560\n\tbuffer_load_dwordx4 v[236:239], v213, s[24:27], s65 offen\n\tv_mfma_f32_16x16x32_bf16 v[28:31], a[84:87], a[220:223], v[28:31]\n\tv_mfma_f32_16x16x32_bf16 v[24:27], a[80:83], a[220:223], v[24:27]\n\tv_mfma_f32_16x16x32_bf16 v[20:23], a[76:79], a[220:223], v[20:23]\n\tv_mfma_f32_16x16x32_bf16 v[16:19], a[72:75], a[220:223], v[16:19]\n\ts_waitcnt lgkmcnt(1)\n\tds_read_b128 a[220:223], v203 offset:7168\n\tv_mfma_f32_16x16x32_bf16 v[32:35], a[72:75], a[224:227], v[32:35]\n\ts_waitcnt vmcnt(15)\n\ts_barrier\n\tv_mfma_f32_16x16x32_bf16 v[36:39], a[76:79], a[224:227], v[36:39]\n\tbuffer_load_dwordx4 v202, s[20:23], s38 offen offset:0 lds\n\tv_mfma_f32_16x16x32_bf16 v[40:43], a[80:83], a[224:227], v[40:43]\n\tds_read_b64_tr_b16 a[96:97], v198 offset:0\n\tv_mfma_f32_16x16x32_bf16 v[44:47], a[84:87], a[224:227], v[44:47]\n\tds_read_b64_tr_b16 a[98:99], v198 offset:256\n\tv_mfma_f32_16x16x32_bf16 v[60:63], a[84:87], a[228:231], v[60:63]\n\ts_add_u32 m0, s39, 37824\n\tds_read_b64_tr_b16 a[100:101], v199 offset:4096\n\tv_mfma_f32_16x16x32_bf16 v[56:59], a[80:83], a[228:231], v[56:59]\n\tbuffer_load_dwordx4 v202, s[20:23], s38 offen offset:64 lds\n\tv_mfma_f32_16x16x32_bf16 v[52:55], a[76:79], a[228:231], v[52:55]\n\tds_read_b64_tr_b16 a[102:103], v199 offset:4352\n\tv_mfma_f32_16x16x32_bf16 v[48:51], a[72:75], a[228:231], v[48:51]\n\tds_read_b64_tr_b16 a[104:105], v199 offset:0\n\tv_mfma_f32_16x16x32_bf16 v[64:67], a[72:75], a[232:235], v[64:67]\n\ts_add_u32 m0, s39, 38784\n\tds_read_b64_tr_b16 a[106:107], v199 offset:256\n\tv_mfma_f32_16x16x32_bf16 v[68:71], a[76:79], a[232:235], v[68:71]\n\tbuffer_load_dwordx4 v202, s[20:23], s38 offen offset:128 lds\n\tv_mfma_f32_16x16x32_bf16 v[72:75], a[80:83], a[232:235], v[72:75]\n\tds_read_b64_tr_b16 a[108:109], v198 offset:5120\n\tv_mfma_f32_16x16x32_bf16 v[76:79], a[84:87], a[232:235], v[76:79]\n\tds_read_b64_tr_b16 a[110:111], v198 offset:5376\n\tv_mfma_f32_16x16x32_bf16 v[92:95], a[84:87], a[236:239], v[92:95]\n\ts_add_u32 m0, s39, 39744\n\tds_read_b64_tr_b16 a[112:113], v200 offset:4096\n\tv_mfma_f32_16x16x32_bf16 v[88:91], a[80:83], a[236:239], v[88:91]\n\tbuffer_load_dwordx4 v202, s[20:23], s38 offen offset:192 lds\n\tv_mfma_f32_16x16x32_bf16 v[84:87], a[76:79], a[236:239], v[84:87]\n\tds_read_b64_tr_b16 a[114:115], v200 offset:4352\n\tv_mfma_f32_16x16x32_bf16 v[80:83], a[72:75], a[236:239], v[80:83]\n\ts_waitcnt lgkmcnt(4)\n\tds_read_b64_tr_b16 a[116:117], v201 offset:8192\n\tv_mfma_f32_16x16x32_bf16 v[0:3], a[96:99], a[216:219], v[0:3]\n\ts_add_u32 m0, s39, 40704\n\tds_read_b64_tr_b16 a[118:119], v201 offset:8448\n\tv_mfma_f32_16x16x32_bf16 v[4:7], a[100:103], a[216:219], v[4:7]\n\tbuffer_load_dwordx4 v202, s[20:23], s38 offen offset:256 lds\n\tv_mfma_f32_16x16x32_bf16 v[8:11], a[104:107], a[216:219], v[8:11]\n\ts_waitcnt lgkmcnt(3)\n\tds_read_b128 a[224:227], v203 offset:11776\n\tv_mfma_f32_16x16x32_bf16 v[12:15], a[108:111], a[216:219], v[12:15]\n\tds_read_b128 a[228:231], v203 offset:16384\n\tv_mfma_f32_16x16x32_bf16 v[28:31], a[108:111], a[220:223], v[28:31]\n\ts_waitcnt lgkmcnt(4)\n\ts_add_u32 m0, s39, 41664\n\tds_read_b128 a[232:235], v203 offset:20992\n\tv_mfma_f32_16x16x32_bf16 v[128:131], a[112:115], a[248:251], v[128:131]\n\tds_read_b128 a[236:239], v203 offset:25600\n\tv_mfma_f32_16x16x32_bf16 v[24:27], a[104:107], a[220:223], v[24:27]\n\ts_waitcnt lgkmcnt(4)\n\tds_read_b64_tr_b16 a[120:121], v198 offset:18432\n\tv_mfma_f32_16x16x32_bf16 v[132:135], a[116:119], a[248:251], v[132:135]\n\tbuffer_load_dwordx4 v202, s[20:23], s38 offen offset:320 lds\n\tv_mfma_f32_16x16x32_bf16 v[20:23], a[100:103], a[220:223], v[20:23]\n\tds_read_b64_tr_b16 a[122:123], v198 offset:18688\n\tv_mfma_f32_16x16x32_bf16 v[16:19], a[96:99], a[220:223], v[16:19]\n\ts_waitcnt lgkmcnt(4)\n\tds_read_b64_tr_b16 a[124:125], v199 offset:22528\n\tv_mfma_f32_16x16x32_bf16 v[32:35], a[96:99], a[224:227], v[32:35]\n\ts_add_u32 m0, s39, 42624\n\tds_read_b64_tr_b16 a[126:127], v199 offset:22784\n\tv_mfma_f32_16x16x32_bf16 v[36:39], a[100:103], a[224:227], v[36:39]\n\tbuffer_load_dwordx4 v202, s[20:23], s38 offen offset:384 lds\n\tv_mfma_f32_16x16x32_bf16 v[40:43], a[104:107], a[224:227], v[40:43]\n\tds_read_b64_tr_b16 a[128:129], v199 offset:18432\n\tv_mfma_f32_16x16x32_bf16 v[44:47], a[108:111], a[224:227], v[44:47]\n\tds_read_b64_tr_b16 a[130:131], v199 offset:18688\n\tv_mfma_f32_16x16x32_bf16 v[60:63], a[108:111], a[228:231], v[60:63]\n\ts_add_u32 m0, s39, 43584\n\tds_read_b64_tr_b16 a[132:133], v198 offset:23552\n\tv_mfma_f32_16x16x32_bf16 v[56:59], a[104:107], a[228:231], v[56:59]\n\tbuffer_load_dwordx4 v202, s[20:23], s38 offen offset:448 lds\n\tv_mfma_f32_16x16x32_bf16 v[52:55], a[100:103], a[228:231], v[52:55]\n\tds_read_b64_tr_b16 a[134:135], v198 offset:23808\n\tv_mfma_f32_16x16x32_bf16 v[48:51], a[96:99], a[228:231], v[48:51]\n\ts_waitcnt lgkmcnt(4)\n\tds_read_b64_tr_b16 a[136:137], v200 offset:22528\n\tv_mfma_f32_16x16x32_bf16 v[64:67], a[96:99], a[232:235], v[64:67]\n\ts_add_u32 m0, s39, 44544\n\tds_read_b64_tr_b16 a[138:139], v200 offset:22784\n\tv_mfma_f32_16x16x32_bf16 v[68:71], a[100:103], a[232:235], v[68:71]\n\tbuffer_load_dwordx4 v202, s[20:23], s38 offen offset:512 lds\n\ts_add_u32 s38, s38, 36864\n\tv_mfma_f32_16x16x32_bf16 v[72:75], a[104:107], a[232:235], v[72:75]\n\tds_read_b64_tr_b16 a[140:141], v201 offset:26624\n\tv_mfma_f32_16x16x32_bf16 v[76:79], a[108:111], a[232:235], v[76:79]\n\tds_read_b64_tr_b16 a[142:143], v201 offset:26880\n\tv_mfma_f32_16x16x32_bf16 v[92:95], a[108:111], a[236:239], v[92:95]\n\ts_add_u32 m0, s39, 73728\n\tds_read_b128 a[248:251], v193 offset:1760\n\tv_mfma_f32_16x16x32_bf16 v[88:91], a[104:107], a[236:239], v[88:91]\n\tds_read_b128 a[216:219], v192 offset:1760\n\tv_mfma_f32_16x16x32_bf16 v[84:87], a[100:103], a[236:239], v[84:87]\n\tds_read_b128 a[220:223], v192 offset:6368\n\tv_mfma_f32_16x16x32_bf16 v[80:83], a[96:99], a[236:239], v[80:83]\n\tds_read_b128 a[224:227], v192 offset:10976\n\tds_read_b128 a[228:231], v192 offset:15584\n\tds_read_b128 a[232:235], v192 offset:20192\n\ts_waitcnt lgkmcnt(5)\n\tv_mfma_f32_16x16x32_bf16 v[128:131], a[136:139], a[248:251], v[128:131]\n\ts_waitcnt vmcnt(13)\n\ts_barrier\n\tds_read_b128 a[236:239], v192 offset:24800\n\tv_mfma_f32_16x16x32_bf16 v[132:135], a[140:143], a[248:251], v[132:135]\n\ts_waitcnt lgkmcnt(4)\n\tbuffer_load_dwordx4 v202, s[20:23], s38 offen offset:0 lds\n\tv_mfma_f32_16x16x32_bf16 v[0:3], a[120:123], a[216:219], v[0:3]\n\tds_read_b64_tr_b16 a[144:145], v194 offset:0\n\tv_mfma_f32_16x16x32_bf16 v[4:7], a[124:127], a[216:219], v[4:7]\n\tds_read_b64_tr_b16 a[146:147], v194 offset:256\n\tv_mfma_f32_16x16x32_bf16 v[8:11], a[128:131], a[216:219], v[8:11]\n\ts_add_u32 m0, s39, 74688\n\tds_read_b64_tr_b16 a[148:149], v195 offset:4096\n\tv_mfma_f32_16x16x32_bf16 v[12:15], a[132:135], a[216:219], v[12:15]\n\tbuffer_load_dwordx4 v202, s[20:23], s38 offen offset:64 lds\n\tv_mfma_f32_16x16x32_bf16 v[28:31], a[132:135], a[220:223], v[28:31]\n\tds_read_b64_tr_b16 a[150:151], v195 offset:4352\n\tv_mfma_f32_16x16x32_bf16 v[24:27], a[128:131], a[220:223], v[24:27]\n\tds_read_b64_tr_b16 a[152:153], v195 offset:0\n\tv_mfma_f32_16x16x32_bf16 v[20:23], a[124:127], a[220:223], v[20:23]\n\ts_add_u32 m0, s39, 75648\n\tds_read_b64_tr_b16 a[154:155], v195 offset:256\n\tv_mfma_f32_16x16x32_bf16 v[16:19], a[120:123], a[220:223], v[16:19]\n\ts_waitcnt lgkmcnt(4)\n\tds_read_b64_tr_b16 a[156:157], v194 offset:5120\n\tv_mfma_f32_16x16x32_bf16 v[32:35], a[120:123], a[224:227], v[32:35]\n\tbuffer_load_dwordx4 v202, s[20:23], s38 offen offset:128 lds\n\tv_mfma_f32_16x16x32_bf16 v[36:39], a[124:127], a[224:227], v[36:39]\n\tds_read_b64_tr_b16 a[158:159], v194 offset:5376\n\tv_mfma_f32_16x16x32_bf16 v[40:43], a[128:131], a[224:227], v[40:43]\n\tds_read_b64_tr_b16 a[160:161], v196 offset:4096\n\tv_mfma_f32_16x16x32_bf16 v[44:47], a[132:135], a[224:227], v[44:47]\n\ts_add_u32 m0, s39, 76608\n\tds_read_b64_tr_b16 a[162:163], v196 offset:4352\n\tv_mfma_f32_16x16x32_bf16 v[60:63], a[132:135], a[228:231], v[60:63]\n\tds_read_b64_tr_b16 a[164:165], v197 offset:8192\n\tv_mfma_f32_16x16x32_bf16 v[56:59], a[128:131], a[228:231], v[56:59]\n\tds_read_b64_tr_b16 a[166:167], v197 offset:8448\n\tv_mfma_f32_16x16x32_bf16 v[52:55], a[124:127], a[228:231], v[52:55]\n\tbuffer_load_dwordx4 v202, s[20:23], s38 offen offset:192 lds\n\tv_mfma_f32_16x16x32_bf16 v[48:51], a[120:123], a[228:231], v[48:51]\n\tds_read_b128 a[248:251], v193 offset:1824\n\tv_mfma_f32_16x16x32_bf16 v[64:67], a[120:123], a[232:235], v[64:67]\n\tds_read_b128 a[216:219], v192 offset:1824\n\tv_mfma_f32_16x16x32_bf16 v[68:71], a[124:127], a[232:235], v[68:71]\n\ts_add_u32 m0, s39, 77568\n\tds_read_b128 a[220:223], v192 offset:6432\n\tv_mfma_f32_16x16x32_bf16 v[72:75], a[128:131], a[232:235], v[72:75]\n\tds_read_b128 a[224:227], v192 offset:11040\n\tv_mfma_f32_16x16x32_bf16 v[76:79], a[132:135], a[232:235], v[76:79]\n\tbuffer_load_dwordx4 v202, s[20:23], s38 offen offset:256 lds\n\tv_mfma_f32_16x16x32_bf16 v[92:95], a[132:135], a[236:239], v[92:95]\n\tds_read_b128 a[228:231], v192 offset:15648\n\tv_mfma_f32_16x16x32_bf16 v[88:91], a[128:131], a[236:239], v[88:91]\n\tds_read_b128 a[232:235], v192 offset:20256\n\tv_mfma_f32_16x16x32_bf16 v[84:87], a[124:127], a[236:239], v[84:87]\n\ts_add_u32 m0, s39, 78528\n\tds_read_b128 a[252:255], v193 offset:20320\n\tv_mfma_f32_16x16x32_bf16 v[80:83], a[120:123], a[236:239], v[80:83]\n\ts_waitcnt lgkmcnt(4)\n\tds_read_b128 a[236:239], v192 offset:24864\n\tv_mfma_f32_16x16x32_bf16 v[128:131], a[160:163], a[248:251], v[128:131]\n\tds_read_b64_tr_b16 v[168:169], v194 offset:18432\n\tv_mfma_f32_16x16x32_bf16 v[132:135], a[164:167], a[248:251], v[132:135]\n\tbuffer_load_dwordx4 v202, s[20:23], s38 offen offset:320 lds\n\tv_mfma_f32_16x16x32_bf16 v[0:3], a[144:147], a[216:219], v[0:3]\n\tds_read_b64_tr_b16 v[170:171], v194 offset:18688\n\tv_mfma_f32_16x16x32_bf16 v[4:7], a[148:151], a[216:219], v[4:7]\n\tds_read_b64_tr_b16 v[172:173], v195 offset:22528\n\tv_mfma_f32_16x16x32_bf16 v[8:11], a[152:155], a[216:219], v[8:11]\n\ts_add_u32 m0, s39, 79488\n\tds_read_b64_tr_b16 v[174:175], v195 offset:22784\n\tv_mfma_f32_16x16x32_bf16 v[12:15], a[156:159], a[216:219], v[12:15]\n\tds_read_b64_tr_b16 v[176:177], v195 offset:18432\n\tv_mfma_f32_16x16x32_bf16 v[28:31], a[156:159], a[220:223], v[28:31]\n\tbuffer_load_dwordx4 v202, s[20:23], s38 offen offset:384 lds\n\tv_mfma_f32_16x16x32_bf16 v[24:27], a[152:155], a[220:223], v[24:27]\n\tds_read_b64_tr_b16 v[178:179], v195 offset:18688\n\tv_mfma_f32_16x16x32_bf16 v[20:23], a[148:151], a[220:223], v[20:23]\n\tds_read_b64_tr_b16 v[180:181], v194 offset:23552\n\tv_mfma_f32_16x16x32_bf16 v[16:19], a[144:147], a[220:223], v[16:19]\n\ts_waitcnt lgkmcnt(4)\n\ts_add_u32 m0, s39, 80448\n\tds_read_b64_tr_b16 v[182:183], v194 offset:23808\n\tv_mfma_f32_16x16x32_bf16 v[32:35], a[144:147], a[224:227], v[32:35]\n\tds_read_b64_tr_b16 v[184:185], v196 offset:22528\n\tv_mfma_f32_16x16x32_bf16 v[36:39], a[148:151], a[224:227], v[36:39]\n\tds_read_b64_tr_b16 v[186:187], v196 offset:22784\n\tv_mfma_f32_16x16x32_bf16 v[40:43], a[152:155], a[224:227], v[40:43]\n\tbuffer_load_dwordx4 v202, s[20:23], s38 offen offset:448 lds\n\tv_mfma_f32_16x16x32_bf16 v[44:47], a[156:159], a[224:227], v[44:47]\n\tds_read_b64_tr_b16 v[188:189], v197 offset:26624\n\tv_mfma_f32_16x16x32_bf16 v[60:63], a[156:159], a[228:231], v[60:63]\n\tds_read_b64_tr_b16 v[190:191], v197 offset:26880\n\tv_mfma_f32_16x16x32_bf16 v[56:59], a[152:155], a[228:231], v[56:59]\n\ts_add_u32 m0, s39, 81408\n\tds_read_b128 a[248:251], v193 offset:1888\n\tv_mfma_f32_16x16x32_bf16 v[52:55], a[148:151], a[228:231], v[52:55]\n\tds_read_b128 a[216:219], v192 offset:1888\n\tv_mfma_f32_16x16x32_bf16 v[48:51], a[144:147], a[228:231], v[48:51]\n\tbuffer_load_dwordx4 v202, s[20:23], s38 offen offset:512 lds\n\ts_add_u32 s38, s38, 36864\n\tv_mfma_f32_16x16x32_bf16 v[64:67], a[144:147], a[232:235], v[64:67]\n\tds_read_b128 a[220:223], v192 offset:6496\n\tv_mfma_f32_16x16x32_bf16 v[68:71], a[148:151], a[232:235], v[68:71]\n\tds_read_b128 a[224:227], v192 offset:11104\n\tv_mfma_f32_16x16x32_bf16 v[72:75], a[152:155], a[232:235], v[72:75]\n\ts_add_u32 m0, s39, 0\n\tds_read_b128 a[228:231], v192 offset:15712\n\tv_mfma_f32_16x16x32_bf16 v[76:79], a[156:159], a[232:235], v[76:79]\n\tds_read_b128 a[232:235], v192 offset:20320\n\tv_mfma_f32_16x16x32_bf16 v[92:95], a[156:159], a[236:239], v[92:95]\n\tds_read_b128 a[240:243], v192 offset:29536\n\tv_mfma_f32_16x16x32_bf16 v[88:91], a[152:155], a[236:239], v[88:91]\n\tds_read_b128 a[244:247], v192 offset:34144\n\tv_mfma_f32_16x16x32_bf16 v[84:87], a[148:151], a[236:239], v[84:87]\n\tv_mfma_f32_16x16x32_bf16 v[80:83], a[144:147], a[236:239], v[80:83]\n\ts_waitcnt vmcnt(9) lgkmcnt(4)\n\ts_barrier\n\tds_read_b128 a[236:239], v192 offset:24928\n\tv_mfma_f32_16x16x32_bf16 v[128:131], v[184:187], a[248:251], v[128:131]\n\tds_read_b64_tr_b16 v[144:145], v194 offset:36864\n\tv_mfma_f32_16x16x32_bf16 v[132:135], v[188:191], a[248:251], v[132:135]\n\tds_read_b64_tr_b16 v[146:147], v194 offset:37120\n\tv_mfma_f32_16x16x32_bf16 v[0:3], v[168:171], a[216:219], v[0:3]\n\tds_read_b64_tr_b16 v[148:149], v195 offset:40960\n\tv_mfma_f32_16x16x32_bf16 v[4:7], v[172:175], a[216:219], v[4:7]\n\tds_read_b64_tr_b16 v[150:151], v195 offset:41216\n\tv_mfma_f32_16x16x32_bf16 v[8:11], v[176:179], a[216:219], v[8:11]\n\tds_read_b64_tr_b16 v[152:153], v195 offset:36864\n\tv_mfma_f32_16x16x32_bf16 v[12:15], v[180:183], a[216:219], v[12:15]\n\tds_read_b64_tr_b16 v[154:155], v195 offset:37120\n\tv_mfma_f32_16x16x32_bf16 v[28:31], v[180:183], a[220:223], v[28:31]\n\tds_read_b64_tr_b16 v[156:157], v194 offset:41984\n\tv_mfma_f32_16x16x32_bf16 v[24:27], v[176:179], a[220:223], v[24:27]\n\tbuffer_load_dwordx4 v202, s[20:23], s38 offen offset:0 lds\n\tv_mfma_f32_16x16x32_bf16 v[20:23], v[172:175], a[220:223], v[20:23]\n\tds_read_b64_tr_b16 v[158:159], v194 offset:42240\n\tv_mfma_f32_16x16x32_bf16 v[16:19], v[168:171], a[220:223], v[16:19]\n\tds_read_b64_tr_b16 v[160:161], v196 offset:40960\n\tv_mfma_f32_16x16x32_bf16 v[32:35], v[168:171], a[224:227], v[32:35]\n\ts_add_u32 m0, s39, 960\n\tds_read_b64_tr_b16 v[162:163], v196 offset:41216\n\tv_mfma_f32_16x16x32_bf16 v[36:39], v[172:175], a[224:227], v[36:39]\n\tds_read_b64_tr_b16 v[164:165], v197 offset:45056\n\tv_mfma_f32_16x16x32_bf16 v[40:43], v[176:179], a[224:227], v[40:43]\n\tds_read_b64_tr_b16 v[166:167], v197 offset:45312\n\tv_mfma_f32_16x16x32_bf16 v[44:47], v[180:183], a[224:227], v[44:47]\n\ts_waitcnt lgkmcnt(5)\n\tds_read_b128 a[248:251], v193 offset:1952\n\tv_mfma_f32_16x16x32_bf16 v[60:63], v[180:183], a[228:231], v[60:63]\n\tds_read_b128 a[216:219], v192 offset:1952\n\tv_mfma_f32_16x16x32_bf16 v[56:59], v[176:179], a[228:231], v[56:59]\n\tds_read_b128 a[220:223], v192 offset:6560\n\tv_mfma_f32_16x16x32_bf16 v[52:55], v[172:175], a[228:231], v[52:55]\n\tbuffer_load_dwordx4 v202, s[20:23], s38 offen offset:64 lds\n\tv_mfma_f32_16x16x32_bf16 v[48:51], v[168:171], a[228:231], v[48:51]\n\tds_read_b128 a[224:227], v192 offset:11168\n\tv_mfma_f32_16x16x32_bf16 v[136:139], v[184:187], a[252:255], 0\n\tds_read_b128 a[228:231], v192 offset:15776\n\tv_mfma_f32_16x16x32_bf16 v[140:143], v[188:191], a[252:255], 0\n\ts_add_u32 m0, s39, 1920\n\tds_read_b128 a[252:255], v193 offset:20384\n\tv_mfma_f32_16x16x32_bf16 v[64:67], v[168:171], a[232:235], v[64:67]\n\tv_mfma_f32_16x16x32_bf16 v[68:71], v[172:175], a[232:235], v[68:71]\n\tv_mfma_f32_16x16x32_bf16 v[72:75], v[176:179], a[232:235], v[72:75]\n\tv_mfma_f32_16x16x32_bf16 v[76:79], v[180:183], a[232:235], v[76:79]\n\tds_read_b128 a[232:235], v192 offset:20384\n\tv_mfma_f32_16x16x32_bf16 v[92:95], v[180:183], a[236:239], v[92:95]\n\tbuffer_load_dwordx4 v202, s[20:23], s38 offen offset:128 lds\n\tv_mfma_f32_16x16x32_bf16 v[88:91], v[176:179], a[236:239], v[88:91]\n\tv_mfma_f32_16x16x32_bf16 v[84:87], v[172:175], a[236:239], v[84:87]\n\tv_mfma_f32_16x16x32_bf16 v[80:83], v[168:171], a[236:239], v[80:83]\n\ts_add_u32 m0, s39, 2880\n\tds_read_b128 a[236:239], v192 offset:24992\n\tv_mfma_f32_16x16x32_bf16 v[96:99], v[168:171], a[240:243], 0\n\tv_mfma_f32_16x16x32_bf16 v[100:103], v[172:175], a[240:243], 0\n\tv_mfma_f32_16x16x32_bf16 v[104:107], v[176:179], a[240:243], 0\n\tv_mfma_f32_16x16x32_bf16 v[108:111], v[180:183], a[240:243], 0\n\tds_read_b128 a[240:243], v192 offset:29600\n\tv_mfma_f32_16x16x32_bf16 v[124:127], v[180:183], a[244:247], 0\n\tv_mfma_f32_16x16x32_bf16 v[120:123], v[176:179], a[244:247], 0\n\tbuffer_load_dwordx4 v202, s[20:23], s38 offen offset:192 lds\n\tv_mfma_f32_16x16x32_bf16 v[116:119], v[172:175], a[244:247], 0\n\tv_mfma_f32_16x16x32_bf16 v[112:115], v[168:171], a[244:247], 0\n\ts_waitcnt lgkmcnt(1)\n\tds_read_b128 a[244:247], v192 offset:34208\n\tv_mfma_f32_16x16x32_bf16 v[128:131], v[160:163], a[248:251], v[128:131]\n\ts_add_u32 m0, s39, 3840\n\tds_read_b64_tr_b16 v[168:169], v194 offset:55296\n\tv_mfma_f32_16x16x32_bf16 v[132:135], v[164:167], a[248:251], v[132:135]\n\tds_read_b64_tr_b16 v[170:171], v194 offset:55552\n\tv_mfma_f32_16x16x32_bf16 v[0:3], v[144:147], a[216:219], v[0:3]\n\tds_read_b64_tr_b16 v[172:173], v195 offset:59392\n\tv_mfma_f32_16x16x32_bf16 v[4:7], v[148:151], a[216:219], v[4:7]\n\tds_read_b64_tr_b16 v[174:175], v195 offset:59648\n\tv_mfma_f32_16x16x32_bf16 v[8:11], v[152:155], a[216:219], v[8:11]\n\tds_read_b64_tr_b16 v[176:177], v195 offset:55296\n\tv_mfma_f32_16x16x32_bf16 v[12:15], v[156:159], a[216:219], v[12:15]\n\tds_read_b64_tr_b16 v[178:179], v195 offset:55552\n\tv_mfma_f32_16x16x32_bf16 v[28:31], v[156:159], a[220:223], v[28:31]\n\tbuffer_load_dwordx4 v202, s[20:23], s38 offen offset:256 lds\n\tv_mfma_f32_16x16x32_bf16 v[24:27], v[152:155], a[220:223], v[24:27]\n\tds_read_b64_tr_b16 v[180:181], v194 offset:60416\n\tv_mfma_f32_16x16x32_bf16 v[20:23], v[148:151], a[220:223], v[20:23]\n\tds_read_b64_tr_b16 v[182:183], v194 offset:60672\n\tv_mfma_f32_16x16x32_bf16 v[16:19], v[144:147], a[220:223], v[16:19]\n\ts_add_u32 m0, s39, 4800\n\tds_read_b64_tr_b16 v[184:185], v196 offset:59392\n\tv_mfma_f32_16x16x32_bf16 v[32:35], v[144:147], a[224:227], v[32:35]\n\tds_read_b64_tr_b16 v[186:187], v196 offset:59648\n\tv_mfma_f32_16x16x32_bf16 v[36:39], v[148:151], a[224:227], v[36:39]\n\tds_read_b64_tr_b16 v[188:189], v197 offset:63488\n\tv_mfma_f32_16x16x32_bf16 v[40:43], v[152:155], a[224:227], v[40:43]\n\tds_read_b64_tr_b16 v[190:191], v197 offset:63744\n\tv_mfma_f32_16x16x32_bf16 v[44:47], v[156:159], a[224:227], v[44:47]\n\tds_read_b128 a[248:251], v193 offset:1152\n\tv_mfma_f32_16x16x32_bf16 v[60:63], v[156:159], a[228:231], v[60:63]\n\tds_read_b128 a[216:219], v192 offset:1152\n\tv_mfma_f32_16x16x32_bf16 v[56:59], v[152:155], a[228:231], v[56:59]\n\tbuffer_load_dwordx4 v202, s[20:23], s38 offen offset:320 lds\n\tv_mfma_f32_16x16x32_bf16 v[52:55], v[148:151], a[228:231], v[52:55]\n\tds_read_b128 a[220:223], v192 offset:5760\n\tv_mfma_f32_16x16x32_bf16 v[48:51], v[144:147], a[228:231], v[48:51]\n\tds_read_b128 a[224:227], v192 offset:10368\n\tv_mfma_f32_16x16x32_bf16 v[136:139], v[160:163], a[252:255], v[136:139]\n\ts_add_u32 m0, s39, 5760\n\tds_read_b128 a[228:231], v192 offset:14976\n\tv_mfma_f32_16x16x32_bf16 v[140:143], v[164:167], a[252:255], v[140:143]\n\tds_read_b128 a[252:255], v193 offset:19584\n\tv_mfma_f32_16x16x32_bf16 v[64:67], v[144:147], a[232:235], v[64:67]\n\tv_mfma_f32_16x16x32_bf16 v[68:71], v[148:151], a[232:235], v[68:71]\n\tv_mfma_f32_16x16x32_bf16 v[72:75], v[152:155], a[232:235], v[72:75]\n\tv_mfma_f32_16x16x32_bf16 v[76:79], v[156:159], a[232:235], v[76:79]\n\tds_read_b128 a[232:235], v192 offset:19584\n\tv_mfma_f32_16x16x32_bf16 v[92:95], v[156:159], a[236:239], v[92:95]\n\tbuffer_load_dwordx4 v202, s[20:23], s38 offen offset:384 lds\n\tv_mfma_f32_16x16x32_bf16 v[88:91], v[152:155], a[236:239], v[88:91]\n\tv_mfma_f32_16x16x32_bf16 v[84:87], v[148:151], a[236:239], v[84:87]\n\tv_mfma_f32_16x16x32_bf16 v[80:83], v[144:147], a[236:239], v[80:83]\n\ts_waitcnt lgkmcnt(1)\n\ts_add_u32 m0, s39, 6720\n\tds_read_b128 a[236:239], v192 offset:24192\n\tv_mfma_f32_16x16x32_bf16 v[96:99], v[144:147], a[240:243], v[96:99]\n\tv_mfma_f32_16x16x32_bf16 v[100:103], v[148:151], a[240:243], v[100:103]\n\tv_mfma_f32_16x16x32_bf16 v[104:107], v[152:155], a[240:243], v[104:107]\n\tv_mfma_f32_16x16x32_bf16 v[108:111], v[156:159], a[240:243], v[108:111]\n\tds_read_b128 a[240:243], v192 offset:28800\n\tv_mfma_f32_16x16x32_bf16 v[124:127], v[156:159], a[244:247], v[124:127]\n\tbuffer_load_dwordx4 v202, s[20:23], s38 offen offset:448 lds\n\tv_mfma_f32_16x16x32_bf16 v[120:123], v[152:155], a[244:247], v[120:123]\n\tv_mfma_f32_16x16x32_bf16 v[116:119], v[148:151], a[244:247], v[116:119]\n\tv_mfma_f32_16x16x32_bf16 v[112:115], v[144:147], a[244:247], v[112:115]\n\ts_waitcnt vmcnt(8)\n\ts_barrier\n\ts_add_u32 m0, s39, 7680\n\tds_read_b128 a[244:247], v192 offset:33408\n\tv_mfma_f32_16x16x32_bf16 v[128:131], v[184:187], a[248:251], v[128:131]\n\tds_read_b64_tr_b16 v[144:145], v198 offset:0\n\tv_mfma_f32_16x16x32_bf16 v[132:135], v[188:191], a[248:251], v[132:135]\n\tds_read_b64_tr_b16 v[146:147], v198 offset:256\n\tv_mfma_f32_16x16x32_bf16 v[0:3], v[168:171], a[216:219], v[0:3]\n\tds_read_b64_tr_b16 v[148:149], v199 offset:4096\n\tv_mfma_f32_16x16x32_bf16 v[4:7], v[172:175], a[216:219], v[4:7]\n\tds_read_b64_tr_b16 v[150:151], v199 offset:4352\n\tv_mfma_f32_16x16x32_bf16 v[8:11], v[176:179], a[216:219], v[8:11]\n\tds_read_b64_tr_b16 v[152:153], v199 offset:0\n\tv_mfma_f32_16x16x32_bf16 v[12:15], v[180:183], a[216:219], v[12:15]\n\tbuffer_load_dwordx4 v202, s[20:23], s38 offen offset:512 lds\n\ts_add_u32 s38, s38, 36864\n\tv_mfma_f32_16x16x32_bf16 v[28:31], v[180:183], a[220:223], v[28:31]\n\tds_read_b64_tr_b16 v[154:155], v199 offset:256\n\tv_mfma_f32_16x16x32_bf16 v[24:27], v[176:179], a[220:223], v[24:27]\n\tds_read_b64_tr_b16 v[156:157], v198 offset:5120\n\tv_mfma_f32_16x16x32_bf16 v[20:23], v[172:175], a[220:223], v[20:23]\n\ts_add_u32 m0, s39, 36864\n\tds_read_b64_tr_b16 v[158:159], v198 offset:5376\n\tv_mfma_f32_16x16x32_bf16 v[16:19], v[168:171], a[220:223], v[16:19]\n\tds_read_b64_tr_b16 v[160:161], v200 offset:4096\n\tv_mfma_f32_16x16x32_bf16 v[32:35], v[168:171], a[224:227], v[32:35]\n\tds_read_b64_tr_b16 v[162:163], v200 offset:4352\n\tv_mfma_f32_16x16x32_bf16 v[36:39], v[172:175], a[224:227], v[36:39]\n\tds_read_b64_tr_b16 v[164:165], v201 offset:8192\n\tv_mfma_f32_16x16x32_bf16 v[40:43], v[176:179], a[224:227], v[40:43]\n\tds_read_b64_tr_b16 v[166:167], v201 offset:8448\n\tv_mfma_f32_16x16x32_bf16 v[44:47], v[180:183], a[224:227], v[44:47]\n\tds_read_b128 a[248:251], v193 offset:1216\n\tv_mfma_f32_16x16x32_bf16 v[60:63], v[180:183], a[228:231], v[60:63]\n\tbuffer_load_dwordx4 v202, s[20:23], s38 offen offset:0 lds\n\tv_mfma_f32_16x16x32_bf16 v[56:59], v[176:179], a[228:231], v[56:59]\n\tds_read_b128 a[216:219], v192 offset:1216\n\tv_mfma_f32_16x16x32_bf16 v[52:55], v[172:175], a[228:231], v[52:55]\n\tds_read_b128 a[220:223], v192 offset:5824\n\tv_mfma_f32_16x16x32_bf16 v[48:51], v[168:171], a[228:231], v[48:51]\n\ts_add_u32 m0, s39, 37824\n\tds_read_b128 a[224:227], v192 offset:10432\n\tv_mfma_f32_16x16x32_bf16 v[136:139], v[184:187], a[252:255], v[136:139]\n\tds_read_b128 a[228:231], v192 offset:15040\n\tv_mfma_f32_16x16x32_bf16 v[140:143], v[188:191], a[252:255], v[140:143]\n\ts_waitcnt lgkmcnt(4)\n\tds_read_b128 a[252:255], v193 offset:19648\n\tv_mfma_f32_16x16x32_bf16 v[64:67], v[168:171], a[232:235], v[64:67]\n\tv_mfma_f32_16x16x32_bf16 v[68:71], v[172:175], a[232:235], v[68:71]\n\tv_mfma_f32_16x16x32_bf16 v[72:75], v[176:179], a[232:235], v[72:75]\n\tbuffer_load_dwordx4 v202, s[20:23], s38 offen offset:64 lds\n\tv_mfma_f32_16x16x32_bf16 v[76:79], v[180:183], a[232:235], v[76:79]\n\tds_read_b128 a[232:235], v192 offset:19648\n\tv_mfma_f32_16x16x32_bf16 v[92:95], v[180:183], a[236:239], v[92:95]\n\tv_mfma_f32_16x16x32_bf16 v[88:91], v[176:179], a[236:239], v[88:91]\n\ts_add_u32 m0, s39, 38784\n\tv_mfma_f32_16x16x32_bf16 v[84:87], v[172:175], a[236:239], v[84:87]\n\tv_mfma_f32_16x16x32_bf16 v[80:83], v[168:171], a[236:239], v[80:83]\n\tds_read_b128 a[236:239], v192 offset:24256\n\tv_mfma_f32_16x16x32_bf16 v[96:99], v[168:171], a[240:243], v[96:99]\n\tv_mfma_f32_16x16x32_bf16 v[100:103], v[172:175], a[240:243], v[100:103]\n\tv_mfma_f32_16x16x32_bf16 v[104:107], v[176:179], a[240:243], v[104:107]\n\tbuffer_load_dwordx4 v202, s[20:23], s38 offen offset:128 lds\n\tv_mfma_f32_16x16x32_bf16 v[108:111], v[180:183], a[240:243], v[108:111]\n\tds_read_b128 a[240:243], v192 offset:28864\n\tv_mfma_f32_16x16x32_bf16 v[124:127], v[180:183], a[244:247], v[124:127]\n\tv_mfma_f32_16x16x32_bf16 v[120:123], v[176:179], a[244:247], v[120:123]\n\ts_add_u32 m0, s39, 39744\n\tv_mfma_f32_16x16x32_bf16 v[116:119], v[172:175], a[244:247], v[116:119]\n\tv_mfma_f32_16x16x32_bf16 v[112:115], v[168:171], a[244:247], v[112:115]\n\tds_read_b128 a[244:247], v192 offset:33472\n\tv_mfma_f32_16x16x32_bf16 v[128:131], v[160:163], a[248:251], v[128:131]\n\tds_read_b64_tr_b16 v[168:169], v198 offset:18432\n\tv_mfma_f32_16x16x32_bf16 v[132:135], v[164:167], a[248:251], v[132:135]\n\ts_waitcnt lgkmcnt(2)\n\tds_read_b64_tr_b16 v[170:171], v198 offset:18688\n\tv_mfma_f32_16x16x32_bf16 v[0:3], v[144:147], a[216:219], v[0:3]\n\tbuffer_load_dwordx4 v202, s[20:23], s38 offen offset:192 lds\n\tv_mfma_f32_16x16x32_bf16 v[4:7], v[148:151], a[216:219], v[4:7]\n\tds_read_b64_tr_b16 v[172:173], v199 offset:22528\n\tv_mfma_f32_16x16x32_bf16 v[8:11], v[152:155], a[216:219], v[8:11]\n\tds_read_b64_tr_b16 v[174:175], v199 offset:22784\n\tv_mfma_f32_16x16x32_bf16 v[12:15], v[156:159], a[216:219], v[12:15]\n\ts_add_u32 m0, s39, 40704\n\tds_read_b64_tr_b16 v[176:177], v199 offset:18432\n\tv_mfma_f32_16x16x32_bf16 v[28:31], v[156:159], a[220:223], v[28:31]\n\tds_read_b64_tr_b16 v[178:179], v199 offset:18688\n\tv_mfma_f32_16x16x32_bf16 v[24:27], v[152:155], a[220:223], v[24:27]\n\tds_read_b64_tr_b16 v[180:181], v198 offset:23552\n\tv_mfma_f32_16x16x32_bf16 v[20:23], v[148:151], a[220:223], v[20:23]\n\tds_read_b64_tr_b16 v[182:183], v198 offset:23808\n\tv_mfma_f32_16x16x32_bf16 v[16:19], v[144:147], a[220:223], v[16:19]\n\tds_read_b64_tr_b16 v[184:185], v200 offset:22528\n\tv_mfma_f32_16x16x32_bf16 v[32:35], v[144:147], a[224:227], v[32:35]\n\tbuffer_load_dwordx4 v202, s[20:23], s38 offen offset:256 lds\n\tv_mfma_f32_16x16x32_bf16 v[36:39], v[148:151], a[224:227], v[36:39]\n\tds_read_b64_tr_b16 v[186:187], v200 offset:22784\n\tv_mfma_f32_16x16x32_bf16 v[40:43], v[152:155], a[224:227], v[40:43]\n\tds_read_b64_tr_b16 v[188:189], v201 offset:26624\n\tv_mfma_f32_16x16x32_bf16 v[44:47], v[156:159], a[224:227], v[44:47]\n\ts_add_u32 m0, s39, 41664\n\tds_read_b64_tr_b16 v[190:191], v201 offset:26880\n\tv_mfma_f32_16x16x32_bf16 v[60:63], v[156:159], a[228:231], v[60:63]\n\tds_read_b128 a[248:251], v193 offset:1280\n\tv_mfma_f32_16x16x32_bf16 v[56:59], v[152:155], a[228:231], v[56:59]\n\tds_read_b128 a[216:219], v192 offset:1280\n\tv_mfma_f32_16x16x32_bf16 v[52:55], v[148:151], a[228:231], v[52:55]\n\tds_read_b128 a[220:223], v192 offset:5888\n\tv_mfma_f32_16x16x32_bf16 v[48:51], v[144:147], a[228:231], v[48:51]\n\tds_read_b128 a[224:227], v192 offset:10496\n\tv_mfma_f32_16x16x32_bf16 v[136:139], v[160:163], a[252:255], v[136:139]\n\tbuffer_load_dwordx4 v202, s[20:23], s38 offen offset:320 lds\n\tv_mfma_f32_16x16x32_bf16 v[140:143], v[164:167], a[252:255], v[140:143]\n\tds_read_b128 a[252:255], v193 offset:19712\n\tv_mfma_f32_16x16x32_bf16 v[64:67], v[144:147], a[232:235], v[64:67]\n\tds_read_b128 a[228:231], v192 offset:15104\n\tv_mfma_f32_16x16x32_bf16 v[68:71], v[148:151], a[232:235], v[68:71]\n\ts_add_u32 m0, s39, 42624\n\tv_mfma_f32_16x16x32_bf16 v[72:75], v[152:155], a[232:235], v[72:75]\n\tv_mfma_f32_16x16x32_bf16 v[76:79], v[156:159], a[232:235], v[76:79]\n\tds_read_b128 a[232:235], v192 offset:19712\n\tv_mfma_f32_16x16x32_bf16 v[92:95], v[156:159], a[236:239], v[92:95]\n\tv_mfma_f32_16x16x32_bf16 v[88:91], v[152:155], a[236:239], v[88:91]\n\tv_mfma_f32_16x16x32_bf16 v[84:87], v[148:151], a[236:239], v[84:87]\n\tbuffer_load_dwordx4 v202, s[20:23], s38 offen offset:384 lds\n\tv_mfma_f32_16x16x32_bf16 v[80:83], v[144:147], a[236:239], v[80:83]\n\tds_read_b128 a[236:239], v192 offset:24320\n\tv_mfma_f32_16x16x32_bf16 v[96:99], v[144:147], a[240:243], v[96:99]\n\tv_mfma_f32_16x16x32_bf16 v[100:103], v[148:151], a[240:243], v[100:103]\n\ts_add_u32 m0, s39, 43584\n\tv_mfma_f32_16x16x32_bf16 v[104:107], v[152:155], a[240:243], v[104:107]\n\tv_mfma_f32_16x16x32_bf16 v[108:111], v[156:159], a[240:243], v[108:111]\n\ts_waitcnt lgkmcnt(1)\n\tds_read_b128 a[240:243], v192 offset:28928\n\tv_mfma_f32_16x16x32_bf16 v[124:127], v[156:159], a[244:247], v[124:127]\n\tv_mfma_f32_16x16x32_bf16 v[120:123], v[152:155], a[244:247], v[120:123]\n\tv_mfma_f32_16x16x32_bf16 v[116:119], v[148:151], a[244:247], v[116:119]\n\tbuffer_load_dwordx4 v202, s[20:23], s38 offen offset:448 lds\n\tv_mfma_f32_16x16x32_bf16 v[112:115], v[144:147], a[244:247], v[112:115]\n\ts_waitcnt vmcnt(8)\n\ts_barrier\n\tds_read_b128 a[244:247], v192 offset:33536\n\tv_mfma_f32_16x16x32_bf16 v[128:131], v[184:187], a[248:251], v[128:131]\n\tds_read_b64_tr_b16 v[144:145], v194 offset:0\n\tv_mfma_f32_16x16x32_bf16 v[132:135], v[188:191], a[248:251], v[132:135]\n\ts_add_u32 m0, s39, 44544\n\tds_read_b64_tr_b16 v[146:147], v194 offset:256\n\tv_mfma_f32_16x16x32_bf16 v[0:3], v[168:171], a[216:219], v[0:3]\n\tds_read_b64_tr_b16 v[148:149], v195 offset:4096\n\tv_mfma_f32_16x16x32_bf16 v[4:7], v[172:175], a[216:219], v[4:7]\n\tds_read_b64_tr_b16 v[150:151], v195 offset:4352\n\tv_mfma_f32_16x16x32_bf16 v[8:11], v[176:179], a[216:219], v[8:11]\n\tds_read_b64_tr_b16 v[152:153], v195 offset:0\n\tv_mfma_f32_16x16x32_bf16 v[12:15], v[180:183], a[216:219], v[12:15]\n\tds_read_b64_tr_b16 v[154:155], v195 offset:256\n\tv_mfma_f32_16x16x32_bf16 v[28:31], v[180:183], a[220:223], v[28:31]\n\tbuffer_load_dwordx4 v202, s[20:23], s38 offen offset:512 lds\n\ts_add_u32 s38, s38, 36864\n\tv_mfma_f32_16x16x32_bf16 v[24:27], v[176:179], a[220:223], v[24:27]\n\tds_read_b64_tr_b16 v[156:157], v194 offset:5120\n\tv_mfma_f32_16x16x32_bf16 v[20:23], v[172:175], a[220:223], v[20:23]\n\tds_read_b64_tr_b16 v[158:159], v194 offset:5376\n\tv_mfma_f32_16x16x32_bf16 v[16:19], v[168:171], a[220:223], v[16:19]\n\ts_add_u32 m0, s39, 73728\n\tds_read_b64_tr_b16 v[160:161], v196 offset:4096\n\tv_mfma_f32_16x16x32_bf16 v[32:35], v[168:171], a[224:227], v[32:35]\n\tds_read_b64_tr_b16 v[162:163], v196 offset:4352\n\tv_mfma_f32_16x16x32_bf16 v[36:39], v[172:175], a[224:227], v[36:39]\n\tds_read_b64_tr_b16 v[164:165], v197 offset:8192\n\tv_mfma_f32_16x16x32_bf16 v[40:43], v[176:179], a[224:227], v[40:43]\n\tds_read_b64_tr_b16 v[166:167], v197 offset:8448\n\tv_mfma_f32_16x16x32_bf16 v[44:47], v[180:183], a[224:227], v[44:47]\n\tds_read_b128 a[248:251], v193 offset:1344\n\tv_mfma_f32_16x16x32_bf16 v[60:63], v[180:183], a[228:231], v[60:63]\n\tbuffer_load_dwordx4 v202, s[20:23], s38 offen offset:0 lds\n\tv_mfma_f32_16x16x32_bf16 v[56:59], v[176:179], a[228:231], v[56:59]\n\tds_read_b128 a[216:219], v192 offset:1344\n\tv_mfma_f32_16x16x32_bf16 v[52:55], v[172:175], a[228:231], v[52:55]\n\tds_read_b128 a[220:223], v192 offset:5952\n\tv_mfma_f32_16x16x32_bf16 v[48:51], v[168:171], a[228:231], v[48:51]\n\ts_add_u32 m0, s39, 74688\n\tds_read_b128 a[224:227], v192 offset:10560\n\tv_mfma_f32_16x16x32_bf16 v[136:139], v[184:187], a[252:255], v[136:139]\n\tds_read_b128 a[228:231], v192 offset:15168\n\tv_mfma_f32_16x16x32_bf16 v[140:143], v[188:191], a[252:255], v[140:143]\n\tds_read_b128 a[252:255], v193 offset:19776\n\tv_mfma_f32_16x16x32_bf16 v[64:67], v[168:171], a[232:235], v[64:67]\n\tv_mfma_f32_16x16x32_bf16 v[68:71], v[172:175], a[232:235], v[68:71]\n\tv_mfma_f32_16x16x32_bf16 v[72:75], v[176:179], a[232:235], v[72:75]\n\tbuffer_load_dwordx4 v202, s[20:23], s38 offen offset:64 lds\n\tv_mfma_f32_16x16x32_bf16 v[76:79], v[180:183], a[232:235], v[76:79]\n\ts_waitcnt lgkmcnt(2)\n\tds_read_b128 a[232:235], v192 offset:19776\n\tv_mfma_f32_16x16x32_bf16 v[92:95], v[180:183], a[236:239], v[92:95]\n\tv_mfma_f32_16x16x32_bf16 v[88:91], v[176:179], a[236:239], v[88:91]\n\ts_add_u32 m0, s39, 75648\n\tv_mfma_f32_16x16x32_bf16 v[84:87], v[172:175], a[236:239], v[84:87]\n\tv_mfma_f32_16x16x32_bf16 v[80:83], v[168:171], a[236:239], v[80:83]\n\tds_read_b128 a[236:239], v192 offset:24384\n\tv_mfma_f32_16x16x32_bf16 v[96:99], v[168:171], a[240:243], v[96:99]\n\tv_mfma_f32_16x16x32_bf16 v[100:103], v[172:175], a[240:243], v[100:103]\n\tv_mfma_f32_16x16x32_bf16 v[104:107], v[176:179], a[240:243], v[104:107]\n\tbuffer_load_dwordx4 v202, s[20:23], s38 offen offset:128 lds\n\tv_mfma_f32_16x16x32_bf16 v[108:111], v[180:183], a[240:243], v[108:111]\n\tds_read_b128 a[240:243], v192 offset:28992\n\tv_mfma_f32_16x16x32_bf16 v[124:127], v[180:183], a[244:247], v[124:127]\n\tv_mfma_f32_16x16x32_bf16 v[120:123], v[176:179], a[244:247], v[120:123]\n\ts_add_u32 m0, s39, 76608\n\tv_mfma_f32_16x16x32_bf16 v[116:119], v[172:175], a[244:247], v[116:119]\n\tv_mfma_f32_16x16x32_bf16 v[112:115], v[168:171], a[244:247], v[112:115]\n\tds_read_b128 a[244:247], v192 offset:33600\n\tv_mfma_f32_16x16x32_bf16 v[128:131], v[160:163], a[248:251], v[128:131]\n\tds_read_b64_tr_b16 v[168:169], v194 offset:18432\n\tv_mfma_f32_16x16x32_bf16 v[132:135], v[164:167], a[248:251], v[132:135]\n\tds_read_b64_tr_b16 v[170:171], v194 offset:18688\n\tv_mfma_f32_16x16x32_bf16 v[0:3], v[144:147], a[216:219], v[0:3]\n\tbuffer_load_dwordx4 v202, s[20:23], s38 offen offset:192 lds\n\tv_mfma_f32_16x16x32_bf16 v[4:7], v[148:151], a[216:219], v[4:7]\n\tds_read_b64_tr_b16 v[172:173], v195 offset:22528\n\tv_mfma_f32_16x16x32_bf16 v[8:11], v[152:155], a[216:219], v[8:11]\n\tds_read_b64_tr_b16 v[174:175], v195 offset:22784\n\tv_mfma_f32_16x16x32_bf16 v[12:15], v[156:159], a[216:219], v[12:15]\n\ts_add_u32 m0, s39, 77568\n\tds_read_b64_tr_b16 v[176:177], v195 offset:18432\n\tv_mfma_f32_16x16x32_bf16 v[28:31], v[156:159], a[220:223], v[28:31]\n\tds_read_b64_tr_b16 v[178:179], v195 offset:18688\n\tv_mfma_f32_16x16x32_bf16 v[24:27], v[152:155], a[220:223], v[24:27]\n\tds_read_b64_tr_b16 v[180:181], v194 offset:23552\n\tv_mfma_f32_16x16x32_bf16 v[20:23], v[148:151], a[220:223], v[20:23]\n\tds_read_b64_tr_b16 v[182:183], v194 offset:23808\n\tv_mfma_f32_16x16x32_bf16 v[16:19], v[144:147], a[220:223], v[16:19]\n\tds_read_b64_tr_b16 v[184:185], v196 offset:22528\n\tv_mfma_f32_16x16x32_bf16 v[32:35], v[144:147], a[224:227], v[32:35]\n\tbuffer_load_dwordx4 v202, s[20:23], s38 offen offset:256 lds\n\tv_mfma_f32_16x16x32_bf16 v[36:39], v[148:151], a[224:227], v[36:39]\n\tds_read_b64_tr_b16 v[186:187], v196 offset:22784\n\tv_mfma_f32_16x16x32_bf16 v[40:43], v[152:155], a[224:227], v[40:43]\n\tds_read_b64_tr_b16 v[188:189], v197 offset:26624\n\tv_mfma_f32_16x16x32_bf16 v[44:47], v[156:159], a[224:227], v[44:47]\n\ts_waitcnt lgkmcnt(4)\n\ts_add_u32 m0, s39, 78528\n\tds_read_b64_tr_b16 v[190:191], v197 offset:26880\n\tv_mfma_f32_16x16x32_bf16 v[60:63], v[156:159], a[228:231], v[60:63]\n\tds_read_b128 a[248:251], v204 offset:1408\n\tv_mfma_f32_16x16x32_bf16 v[56:59], v[152:155], a[228:231], v[56:59]\n\tds_read_b128 a[216:219], v203 offset:1408\n\tv_mfma_f32_16x16x32_bf16 v[52:55], v[148:151], a[228:231], v[52:55]\n\tds_read_b128 a[220:223], v203 offset:6016\n\tv_mfma_f32_16x16x32_bf16 v[48:51], v[144:147], a[228:231], v[48:51]\n\tds_read_b128 a[224:227], v203 offset:10624\n\tv_mfma_f32_16x16x32_bf16 v[136:139], v[160:163], a[252:255], v[136:139]\n\tbuffer_load_dwordx4 v202, s[20:23], s38 offen offset:320 lds\n\tv_mfma_f32_16x16x32_bf16 v[140:143], v[164:167], a[252:255], v[140:143]\n\tds_read_b128 a[252:255], v204 offset:19840\n\tv_mfma_f32_16x16x32_bf16 v[64:67], v[144:147], a[232:235], v[64:67]\n\tds_read_b128 a[228:231], v203 offset:15232\n\tv_mfma_f32_16x16x32_bf16 v[68:71], v[148:151], a[232:235], v[68:71]\n\ts_add_u32 m0, s39, 79488\n\tv_mfma_f32_16x16x32_bf16 v[72:75], v[152:155], a[232:235], v[72:75]\n\tv_mfma_f32_16x16x32_bf16 v[76:79], v[156:159], a[232:235], v[76:79]\n\tds_read_b128 a[232:235], v203 offset:19840\n\tv_mfma_f32_16x16x32_bf16 v[92:95], v[156:159], a[236:239], v[92:95]\n\tv_mfma_f32_16x16x32_bf16 v[88:91], v[152:155], a[236:239], v[88:91]\n\tv_mfma_f32_16x16x32_bf16 v[84:87], v[148:151], a[236:239], v[84:87]\n\tbuffer_load_dwordx4 v202, s[20:23], s38 offen offset:384 lds\n\tv_mfma_f32_16x16x32_bf16 v[80:83], v[144:147], a[236:239], v[80:83]\n\tds_read_b128 a[236:239], v203 offset:24448\n\tv_mfma_f32_16x16x32_bf16 v[96:99], v[144:147], a[240:243], v[96:99]\n\tv_mfma_f32_16x16x32_bf16 v[100:103], v[148:151], a[240:243], v[100:103]\n\ts_add_u32 m0, s39, 80448\n\tv_mfma_f32_16x16x32_bf16 v[104:107], v[152:155], a[240:243], v[104:107]\n\tv_mfma_f32_16x16x32_bf16 v[108:111], v[156:159], a[240:243], v[108:111]\n\tds_read_b128 a[240:243], v203 offset:29056\n\tv_mfma_f32_16x16x32_bf16 v[124:127], v[156:159], a[244:247], v[124:127]\n\tv_mfma_f32_16x16x32_bf16 v[120:123], v[152:155], a[244:247], v[120:123]\n\tv_mfma_f32_16x16x32_bf16 v[116:119], v[148:151], a[244:247], v[116:119]\n\tbuffer_load_dwordx4 v202, s[20:23], s38 offen offset:448 lds\n\tv_mfma_f32_16x16x32_bf16 v[112:115], v[144:147], a[244:247], v[112:115]\n\ts_waitcnt vmcnt(8) lgkmcnt(1)\n\ts_barrier\n\tds_read_b128 a[244:247], v203 offset:33664\n\tv_mfma_f32_16x16x32_bf16 v[128:131], v[184:187], a[248:251], v[128:131]\n\tds_read_b64_tr_b16 v[144:145], v194 offset:36864\n\tv_mfma_f32_16x16x32_bf16 v[132:135], v[188:191], a[248:251], v[132:135]\n\ts_add_u32 m0, s39, 81408\n\tds_read_b64_tr_b16 v[146:147], v194 offset:37120\n\tv_mfma_f32_16x16x32_bf16 v[0:3], v[168:171], a[216:219], v[0:3]\n\tds_read_b64_tr_b16 v[148:149], v195 offset:40960\n\tv_mfma_f32_16x16x32_bf16 v[4:7], v[172:175], a[216:219], v[4:7]\n\tds_read_b64_tr_b16 v[150:151], v195 offset:41216\n\tv_mfma_f32_16x16x32_bf16 v[8:11], v[176:179], a[216:219], v[8:11]\n\tds_read_b64_tr_b16 v[152:153], v195 offset:36864\n\tv_mfma_f32_16x16x32_bf16 v[12:15], v[180:183], a[216:219], v[12:15]\n\tds_read_b64_tr_b16 v[154:155], v195 offset:37120\n\tv_mfma_f32_16x16x32_bf16 v[28:31], v[180:183], a[220:223], v[28:31]\n\tbuffer_load_dwordx4 v202, s[20:23], s38 offen offset:512 lds\n\ts_add_u32 s38, s38, 36864\n\tv_mfma_f32_16x16x32_bf16 v[24:27], v[176:179], a[220:223], v[24:27]\n\tds_read_b64_tr_b16 v[156:157], v194 offset:41984\n\tv_mfma_f32_16x16x32_bf16 v[20:23], v[172:175], a[220:223], v[20:23]\n\tds_read_b64_tr_b16 v[158:159], v194 offset:42240\n\tv_mfma_f32_16x16x32_bf16 v[16:19], v[168:171], a[220:223], v[16:19]\n\ts_add_u32 m0, s39, 0\n\tds_read_b64_tr_b16 v[160:161], v196 offset:40960\n\tv_mfma_f32_16x16x32_bf16 v[32:35], v[168:171], a[224:227], v[32:35]\n\tds_read_b64_tr_b16 v[162:163], v196 offset:41216\n\tv_mfma_f32_16x16x32_bf16 v[36:39], v[172:175], a[224:227], v[36:39]\n\tds_read_b64_tr_b16 v[164:165], v197 offset:45056\n\tv_mfma_f32_16x16x32_bf16 v[40:43], v[176:179], a[224:227], v[40:43]\n\tds_read_b64_tr_b16 v[166:167], v197 offset:45312\n\tv_mfma_f32_16x16x32_bf16 v[44:47], v[180:183], a[224:227], v[44:47]\n\tds_read_b128 a[248:251], v193 offset:608\n\tv_mfma_f32_16x16x32_bf16 v[60:63], v[180:183], a[228:231], v[60:63]\n\tbuffer_load_dwordx4 v202, s[20:23], s38 offen offset:0 lds\n\tv_mfma_f32_16x16x32_bf16 v[56:59], v[176:179], a[228:231], v[56:59]\n\tds_read_b128 a[216:219], v192 offset:608\n\tv_mfma_f32_16x16x32_bf16 v[52:55], v[172:175], a[228:231], v[52:55]\n\tds_read_b128 a[220:223], v192 offset:5216\n\tv_mfma_f32_16x16x32_bf16 v[48:51], v[168:171], a[228:231], v[48:51]\n\ts_add_u32 m0, s39, 960\n\tds_read_b128 a[224:227], v192 offset:9824\n\tv_mfma_f32_16x16x32_bf16 v[136:139], v[184:187], a[252:255], v[136:139]\n\tds_read_b128 a[228:231], v192 offset:14432\n\tv_mfma_f32_16x16x32_bf16 v[140:143], v[188:191], a[252:255], v[140:143]\n\tbuffer_load_dwordx4 v202, s[20:23], s38 offen offset:64 lds\n\tv_mfma_f32_16x16x32_bf16 v[64:67], v[168:171], a[232:235], v[64:67]\n\tds_read_b128 a[252:255], v193 offset:19040\n\tv_mfma_f32_16x16x32_bf16 v[68:71], v[172:175], a[232:235], v[68:71]\n\tv_mfma_f32_16x16x32_bf16 v[72:75], v[176:179], a[232:235], v[72:75]\n\ts_add_u32 m0, s39, 1920\n\tv_mfma_f32_16x16x32_bf16 v[76:79], v[180:183], a[232:235], v[76:79]\n\tbuffer_load_dwordx4 v202, s[20:23], s38 offen offset:128 lds\n\tv_mfma_f32_16x16x32_bf16 v[92:95], v[180:183], a[236:239], v[92:95]\n\tds_read_b128 a[232:235], v192 offset:19040\n\tv_mfma_f32_16x16x32_bf16 v[88:91], v[176:179], a[236:239], v[88:91]\n\tv_mfma_f32_16x16x32_bf16 v[84:87], v[172:175], a[236:239], v[84:87]\n\ts_add_u32 m0, s39, 2880\n\tv_mfma_f32_16x16x32_bf16 v[80:83], v[168:171], a[236:239], v[80:83]\n\ts_waitcnt lgkmcnt(1)\n\tds_read_b128 a[236:239], v192 offset:23648\n\tv_mfma_f32_16x16x32_bf16 v[96:99], v[168:171], a[240:243], v[96:99]\n\tbuffer_load_dwordx4 v202, s[20:23], s38 offen offset:192 lds\n\tv_mfma_f32_16x16x32_bf16 v[100:103], v[172:175], a[240:243], v[100:103]\n\tv_mfma_f32_16x16x32_bf16 v[104:107], v[176:179], a[240:243], v[104:107]\n\tv_mfma_f32_16x16x32_bf16 v[108:111], v[180:183], a[240:243], v[108:111]\n\ts_add_u32 m0, s39, 3840\n\tds_read_b128 a[240:243], v192 offset:28256\n\tv_mfma_f32_16x16x32_bf16 v[124:127], v[180:183], a[244:247], v[124:127]\n\tv_mfma_f32_16x16x32_bf16 v[120:123], v[176:179], a[244:247], v[120:123]\n\tbuffer_load_dwordx4 v202, s[20:23], s38 offen offset:256 lds\n\tv_mfma_f32_16x16x32_bf16 v[116:119], v[172:175], a[244:247], v[116:119]\n\tv_mfma_f32_16x16x32_bf16 v[112:115], v[168:171], a[244:247], v[112:115]\n\tds_read_b128 a[244:247], v192 offset:32864\n\tv_mfma_f32_16x16x32_bf16 v[128:131], v[160:163], a[248:251], v[128:131]\n\ts_add_u32 m0, s39, 4800\n\tds_read_b64_tr_b16 v[168:169], v194 offset:55296\n\tv_mfma_f32_16x16x32_bf16 v[132:135], v[164:167], a[248:251], v[132:135]\n\tds_read_b64_tr_b16 v[170:171], v194 offset:55552\n\tv_mfma_f32_16x16x32_bf16 v[0:3], v[144:147], a[216:219], v[0:3]\n\tbuffer_load_dwordx4 v202, s[20:23], s38 offen offset:320 lds\n\tv_mfma_f32_16x16x32_bf16 v[4:7], v[148:151], a[216:219], v[4:7]\n\tds_read_b64_tr_b16 v[172:173], v195 offset:59392\n\tv_mfma_f32_16x16x32_bf16 v[8:11], v[152:155], a[216:219], v[8:11]\n\tds_read_b64_tr_b16 v[174:175], v195 offset:59648\n\tv_mfma_f32_16x16x32_bf16 v[12:15], v[156:159], a[216:219], v[12:15]\n\ts_add_u32 m0, s39, 5760\n\tds_read_b64_tr_b16 v[176:177], v195 offset:55296\n\tv_mfma_f32_16x16x32_bf16 v[28:31], v[156:159], a[220:223], v[28:31]\n\tds_read_b64_tr_b16 v[178:179], v195 offset:55552\n\tv_mfma_f32_16x16x32_bf16 v[24:27], v[152:155], a[220:223], v[24:27]\n\tbuffer_load_dwordx4 v202, s[20:23], s38 offen offset:384 lds\n\tv_mfma_f32_16x16x32_bf16 v[20:23], v[148:151], a[220:223], v[20:23]\n\tds_read_b64_tr_b16 v[180:181], v194 offset:60416\n\tv_mfma_f32_16x16x32_bf16 v[16:19], v[144:147], a[220:223], v[16:19]\n\tds_read_b64_tr_b16 v[182:183], v194 offset:60672\n\tv_mfma_f32_16x16x32_bf16 v[32:35], v[144:147], a[224:227], v[32:35]\n\ts_add_u32 m0, s39, 6720\n\tds_read_b64_tr_b16 v[184:185], v196 offset:59392\n\tv_mfma_f32_16x16x32_bf16 v[36:39], v[148:151], a[224:227], v[36:39]\n\tds_read_b64_tr_b16 v[186:187], v196 offset:59648\n\tv_mfma_f32_16x16x32_bf16 v[40:43], v[152:155], a[224:227], v[40:43]\n\tbuffer_load_dwordx4 v202, s[20:23], s38 offen offset:448 lds\n\tv_mfma_f32_16x16x32_bf16 v[44:47], v[156:159], a[224:227], v[44:47]\n\tds_read_b64_tr_b16 v[188:189], v197 offset:63488\n\tv_mfma_f32_16x16x32_bf16 v[60:63], v[156:159], a[228:231], v[60:63]\n\tds_read_b64_tr_b16 v[190:191], v197 offset:63744\n\tv_mfma_f32_16x16x32_bf16 v[56:59], v[152:155], a[228:231], v[56:59]\n\ts_add_u32 m0, s39, 7680\n\tds_read_b128 a[248:251], v193 offset:672\n\tv_mfma_f32_16x16x32_bf16 v[52:55], v[148:151], a[228:231], v[52:55]\n\tbuffer_load_dwordx4 v202, s[20:23], s38 offen offset:512 lds\n\ts_add_u32 s38, s38, 36864\n\tv_mfma_f32_16x16x32_bf16 v[48:51], v[144:147], a[228:231], v[48:51]\n\tds_read_b128 a[216:219], v192 offset:672\n\tv_mfma_f32_16x16x32_bf16 v[136:139], v[160:163], a[252:255], v[136:139]\n\tds_read_b128 a[220:223], v192 offset:5280\n\tv_mfma_f32_16x16x32_bf16 v[140:143], v[164:167], a[252:255], v[140:143]\n\ts_waitcnt lgkmcnt(4)\n\ts_add_u32 m0, s39, 36864\n\tds_read_b128 a[252:255], v193 offset:19104\n\tv_mfma_f32_16x16x32_bf16 v[64:67], v[144:147], a[232:235], v[64:67]\n\tds_read_b128 a[224:227], v192 offset:9888\n\tv_mfma_f32_16x16x32_bf16 v[68:71], v[148:151], a[232:235], v[68:71]\n\tds_read_b128 a[228:231], v192 offset:14496\n\tv_mfma_f32_16x16x32_bf16 v[72:75], v[152:155], a[232:235], v[72:75]\n\tv_mfma_f32_16x16x32_bf16 v[76:79], v[156:159], a[232:235], v[76:79]\n\tds_read_b128 a[232:235], v192 offset:19104\n\tv_mfma_f32_16x16x32_bf16 v[92:95], v[156:159], a[236:239], v[92:95]\n\tv_mfma_f32_16x16x32_bf16 v[88:91], v[152:155], a[236:239], v[88:91]\n\tv_mfma_f32_16x16x32_bf16 v[84:87], v[148:151], a[236:239], v[84:87]\n\tv_mfma_f32_16x16x32_bf16 v[80:83], v[144:147], a[236:239], v[80:83]\n\tds_read_b128 a[236:239], v192 offset:23712\n\tv_mfma_f32_16x16x32_bf16 v[96:99], v[144:147], a[240:243], v[96:99]\n\tv_mfma_f32_16x16x32_bf16 v[100:103], v[148:151], a[240:243], v[100:103]\n\tv_mfma_f32_16x16x32_bf16 v[104:107], v[152:155], a[240:243], v[104:107]\n\tv_mfma_f32_16x16x32_bf16 v[108:111], v[156:159], a[240:243], v[108:111]\n\tds_read_b128 a[240:243], v192 offset:28320\n\tv_mfma_f32_16x16x32_bf16 v[124:127], v[156:159], a[244:247], v[124:127]\n\tv_mfma_f32_16x16x32_bf16 v[120:123], v[152:155], a[244:247], v[120:123]\n\tv_mfma_f32_16x16x32_bf16 v[116:119], v[148:151], a[244:247], v[116:119]\n\tv_mfma_f32_16x16x32_bf16 v[112:115], v[144:147], a[244:247], v[112:115]\n\ts_waitcnt vmcnt(9) lgkmcnt(1)\n\ts_barrier\n\tds_read_b128 a[244:247], v192 offset:32928\n\tv_mfma_f32_16x16x32_bf16 v[128:131], v[184:187], a[248:251], v[128:131]\n\tbuffer_load_dwordx4 v202, s[20:23], s38 offen offset:0 lds\n\tv_mfma_f32_16x16x32_bf16 v[132:135], v[188:191], a[248:251], v[132:135]\n\tds_read_b64_tr_b16 v[144:145], v198 offset:0\n\tv_mfma_f32_16x16x32_bf16 v[0:3], v[168:171], a[216:219], v[0:3]\n\tds_read_b64_tr_b16 v[146:147], v198 offset:256\n\tv_mfma_f32_16x16x32_bf16 v[4:7], v[172:175], a[216:219], v[4:7]\n\ts_add_u32 m0, s39, 37824\n\tds_read_b64_tr_b16 v[148:149], v199 offset:4096\n\tv_mfma_f32_16x16x32_bf16 v[8:11], v[176:179], a[216:219], v[8:11]\n\tds_read_b64_tr_b16 v[150:151], v199 offset:4352\n\tv_mfma_f32_16x16x32_bf16 v[12:15], v[180:183], a[216:219], v[12:15]\n\tbuffer_load_dwordx4 v202, s[20:23], s38 offen offset:64 lds\n\tv_mfma_f32_16x16x32_bf16 v[28:31], v[180:183], a[220:223], v[28:31]\n\tds_read_b64_tr_b16 v[152:153], v199 offset:0\n\tv_mfma_f32_16x16x32_bf16 v[24:27], v[176:179], a[220:223], v[24:27]\n\tds_read_b64_tr_b16 v[154:155], v199 offset:256\n\tv_mfma_f32_16x16x32_bf16 v[20:23], v[172:175], a[220:223], v[20:23]\n\ts_add_u32 m0, s39, 38784\n\tds_read_b64_tr_b16 v[156:157], v198 offset:5120\n\tv_mfma_f32_16x16x32_bf16 v[16:19], v[168:171], a[220:223], v[16:19]\n\tds_read_b64_tr_b16 v[158:159], v198 offset:5376\n\tv_mfma_f32_16x16x32_bf16 v[32:35], v[168:171], a[224:227], v[32:35]\n\tbuffer_load_dwordx4 v202, s[20:23], s38 offen offset:128 lds\n\tv_mfma_f32_16x16x32_bf16 v[36:39], v[172:175], a[224:227], v[36:39]\n\tds_read_b64_tr_b16 v[160:161], v200 offset:4096\n\tv_mfma_f32_16x16x32_bf16 v[40:43], v[176:179], a[224:227], v[40:43]\n\tds_read_b64_tr_b16 v[162:163], v200 offset:4352\n\tv_mfma_f32_16x16x32_bf16 v[44:47], v[180:183], a[224:227], v[44:47]\n\ts_add_u32 m0, s39, 39744\n\tds_read_b64_tr_b16 v[164:165], v201 offset:8192\n\tv_mfma_f32_16x16x32_bf16 v[60:63], v[180:183], a[228:231], v[60:63]\n\tds_read_b64_tr_b16 v[166:167], v201 offset:8448\n\tv_mfma_f32_16x16x32_bf16 v[56:59], v[176:179], a[228:231], v[56:59]\n\tbuffer_load_dwordx4 v202, s[20:23], s38 offen offset:192 lds\n\tv_mfma_f32_16x16x32_bf16 v[52:55], v[172:175], a[228:231], v[52:55]\n\tds_read_b128 a[248:251], v193 offset:736\n\tv_mfma_f32_16x16x32_bf16 v[48:51], v[168:171], a[228:231], v[48:51]\n\tds_read_b128 a[216:219], v192 offset:736\n\tv_mfma_f32_16x16x32_bf16 v[136:139], v[184:187], a[252:255], v[136:139]\n\ts_add_u32 m0, s39, 40704\n\tds_read_b128 a[220:223], v192 offset:5344\n\tv_mfma_f32_16x16x32_bf16 v[140:143], v[188:191], a[252:255], v[140:143]\n\tds_read_b128 a[252:255], v193 offset:19168\n\tv_mfma_f32_16x16x32_bf16 v[64:67], v[168:171], a[232:235], v[64:67]\n\tbuffer_load_dwordx4 v202, s[20:23], s38 offen offset:256 lds\n\tv_mfma_f32_16x16x32_bf16 v[68:71], v[172:175], a[232:235], v[68:71]\n\tds_read_b128 a[224:227], v192 offset:9952\n\tv_mfma_f32_16x16x32_bf16 v[72:75], v[176:179], a[232:235], v[72:75]\n\tds_read_b128 a[228:231], v192 offset:14560\n\tv_mfma_f32_16x16x32_bf16 v[76:79], v[180:183], a[232:235], v[76:79]\n\ts_add_u32 m0, s39, 41664\n\tds_read_b128 a[232:235], v192 offset:19168\n\tv_mfma_f32_16x16x32_bf16 v[92:95], v[180:183], a[236:239], v[92:95]\n\tv_mfma_f32_16x16x32_bf16 v[88:91], v[176:179], a[236:239], v[88:91]\n\tv_mfma_f32_16x16x32_bf16 v[84:87], v[172:175], a[236:239], v[84:87]\n\tbuffer_load_dwordx4 v202, s[20:23], s38 offen offset:320 lds\n\tv_mfma_f32_16x16x32_bf16 v[80:83], v[168:171], a[236:239], v[80:83]\n\ts_waitcnt lgkmcnt(2)\n\tds_read_b128 a[236:239], v192 offset:23776\n\tv_mfma_f32_16x16x32_bf16 v[96:99], v[168:171], a[240:243], v[96:99]\n\tv_mfma_f32_16x16x32_bf16 v[100:103], v[172:175], a[240:243], v[100:103]\n\ts_add_u32 m0, s39, 42624\n\tv_mfma_f32_16x16x32_bf16 v[104:107], v[176:179], a[240:243], v[104:107]\n\tv_mfma_f32_16x16x32_bf16 v[108:111], v[180:183], a[240:243], v[108:111]\n\tbuffer_load_dwordx4 v202, s[20:23], s38 offen offset:384 lds\n\tv_mfma_f32_16x16x32_bf16 v[124:127], v[180:183], a[244:247], v[124:127]\n\tds_read_b128 a[240:243], v192 offset:28384\n\tv_mfma_f32_16x16x32_bf16 v[120:123], v[176:179], a[244:247], v[120:123]\n\tv_mfma_f32_16x16x32_bf16 v[116:119], v[172:175], a[244:247], v[116:119]\n\ts_add_u32 m0, s39, 43584\n\tv_mfma_f32_16x16x32_bf16 v[112:115], v[168:171], a[244:247], v[112:115]\n\tds_read_b128 a[244:247], v192 offset:32992\n\tv_mfma_f32_16x16x32_bf16 v[128:131], v[160:163], a[248:251], v[128:131]\n\tbuffer_load_dwordx4 v202, s[20:23], s38 offen offset:448 lds\n\tv_mfma_f32_16x16x32_bf16 v[132:135], v[164:167], a[248:251], v[132:135]\n\tds_read_b64_tr_b16 v[168:169], v198 offset:18432\n\tv_mfma_f32_16x16x32_bf16 v[0:3], v[144:147], a[216:219], v[0:3]\n\tds_read_b64_tr_b16 v[170:171], v198 offset:18688\n\tv_mfma_f32_16x16x32_bf16 v[4:7], v[148:151], a[216:219], v[4:7]\n\ts_add_u32 m0, s39, 44544\n\tds_read_b64_tr_b16 v[172:173], v199 offset:22528\n\tv_mfma_f32_16x16x32_bf16 v[8:11], v[152:155], a[216:219], v[8:11]\n\tds_read_b64_tr_b16 v[174:175], v199 offset:22784\n\tv_mfma_f32_16x16x32_bf16 v[12:15], v[156:159], a[216:219], v[12:15]\n\tbuffer_load_dwordx4 v202, s[20:23], s38 offen offset:512 lds\n\ts_add_u32 s38, s38, 36864\n\tv_mfma_f32_16x16x32_bf16 v[28:31], v[156:159], a[220:223], v[28:31]\n\tds_read_b64_tr_b16 v[176:177], v199 offset:18432\n\tv_mfma_f32_16x16x32_bf16 v[24:27], v[152:155], a[220:223], v[24:27]\n\tds_read_b64_tr_b16 v[178:179], v199 offset:18688\n\tv_mfma_f32_16x16x32_bf16 v[20:23], v[148:151], a[220:223], v[20:23]\n\ts_add_u32 m0, s39, 73728\n\tds_read_b64_tr_b16 v[180:181], v198 offset:23552\n\tv_mfma_f32_16x16x32_bf16 v[16:19], v[144:147], a[220:223], v[16:19]\n\tds_read_b64_tr_b16 v[182:183], v198 offset:23808\n\tv_mfma_f32_16x16x32_bf16 v[32:35], v[144:147], a[224:227], v[32:35]\n\tds_read_b64_tr_b16 v[184:185], v200 offset:22528\n\tv_mfma_f32_16x16x32_bf16 v[36:39], v[148:151], a[224:227], v[36:39]\n\tds_read_b64_tr_b16 v[186:187], v200 offset:22784\n\tv_mfma_f32_16x16x32_bf16 v[40:43], v[152:155], a[224:227], v[40:43]\n\tds_read_b64_tr_b16 v[188:189], v201 offset:26624\n\tv_mfma_f32_16x16x32_bf16 v[44:47], v[156:159], a[224:227], v[44:47]\n\ts_waitcnt lgkmcnt(5)\n\tds_read_b64_tr_b16 v[190:191], v201 offset:26880\n\tv_mfma_f32_16x16x32_bf16 v[60:63], v[156:159], a[228:231], v[60:63]\n\tds_read_b128 a[248:251], v193 offset:800\n\tv_mfma_f32_16x16x32_bf16 v[56:59], v[152:155], a[228:231], v[56:59]\n\tds_read_b128 a[216:219], v192 offset:800\n\tv_mfma_f32_16x16x32_bf16 v[52:55], v[148:151], a[228:231], v[52:55]\n\tds_read_b128 a[220:223], v192 offset:5408\n\tv_mfma_f32_16x16x32_bf16 v[48:51], v[144:147], a[228:231], v[48:51]\n\tds_read_b128 a[224:227], v192 offset:10016\n\tv_mfma_f32_16x16x32_bf16 v[136:139], v[160:163], a[252:255], v[136:139]\n\tds_read_b128 a[228:231], v192 offset:14624\n\tv_mfma_f32_16x16x32_bf16 v[140:143], v[164:167], a[252:255], v[140:143]\n\tds_read_b128 a[252:255], v193 offset:19232\n\tv_mfma_f32_16x16x32_bf16 v[64:67], v[144:147], a[232:235], v[64:67]\n\tv_mfma_f32_16x16x32_bf16 v[68:71], v[148:151], a[232:235], v[68:71]\n\tv_mfma_f32_16x16x32_bf16 v[72:75], v[152:155], a[232:235], v[72:75]\n\tv_mfma_f32_16x16x32_bf16 v[76:79], v[156:159], a[232:235], v[76:79]\n\tds_read_b128 a[232:235], v192 offset:19232\n\tv_mfma_f32_16x16x32_bf16 v[92:95], v[156:159], a[236:239], v[92:95]\n\tv_mfma_f32_16x16x32_bf16 v[88:91], v[152:155], a[236:239], v[88:91]\n\tv_mfma_f32_16x16x32_bf16 v[84:87], v[148:151], a[236:239], v[84:87]\n\tv_mfma_f32_16x16x32_bf16 v[80:83], v[144:147], a[236:239], v[80:83]\n\tds_read_b128 a[236:239], v192 offset:23840\n\tv_mfma_f32_16x16x32_bf16 v[96:99], v[144:147], a[240:243], v[96:99]\n\tv_mfma_f32_16x16x32_bf16 v[100:103], v[148:151], a[240:243], v[100:103]\n\tv_mfma_f32_16x16x32_bf16 v[104:107], v[152:155], a[240:243], v[104:107]\n\tv_mfma_f32_16x16x32_bf16 v[108:111], v[156:159], a[240:243], v[108:111]\n\tds_read_b128 a[240:243], v192 offset:28448\n\tv_mfma_f32_16x16x32_bf16 v[124:127], v[156:159], a[244:247], v[124:127]\n\tv_mfma_f32_16x16x32_bf16 v[120:123], v[152:155], a[244:247], v[120:123]\n\tv_mfma_f32_16x16x32_bf16 v[116:119], v[148:151], a[244:247], v[116:119]\n\tv_mfma_f32_16x16x32_bf16 v[112:115], v[144:147], a[244:247], v[112:115]\n\ts_waitcnt vmcnt(9) lgkmcnt(1)\n\ts_barrier\n\tds_read_b128 a[244:247], v192 offset:33056\n\tv_mfma_f32_16x16x32_bf16 v[128:131], v[184:187], a[248:251], v[128:131]\n\tbuffer_load_dwordx4 v202, s[20:23], s38 offen offset:0 lds\n\tv_mfma_f32_16x16x32_bf16 v[132:135], v[188:191], a[248:251], v[132:135]\n\tds_read_b64_tr_b16 v[144:145], v194 offset:0\n\tv_mfma_f32_16x16x32_bf16 v[0:3], v[168:171], a[216:219], v[0:3]\n\tds_read_b64_tr_b16 v[146:147], v194 offset:256\n\tv_mfma_f32_16x16x32_bf16 v[4:7], v[172:175], a[216:219], v[4:7]\n\ts_add_u32 m0, s39, 74688\n\tds_read_b64_tr_b16 v[148:149], v195 offset:4096\n\tv_mfma_f32_16x16x32_bf16 v[8:11], v[176:179], a[216:219], v[8:11]\n\tbuffer_load_dwordx4 v202, s[20:23], s38 offen offset:64 lds\n\tv_mfma_f32_16x16x32_bf16 v[12:15], v[180:183], a[216:219], v[12:15]\n\tds_read_b64_tr_b16 v[150:151], v195 offset:4352\n\tv_mfma_f32_16x16x32_bf16 v[28:31], v[180:183], a[220:223], v[28:31]\n\tds_read_b64_tr_b16 v[152:153], v195 offset:0\n\tv_mfma_f32_16x16x32_bf16 v[24:27], v[176:179], a[220:223], v[24:27]\n\ts_add_u32 m0, s39, 75648\n\tds_read_b64_tr_b16 v[154:155], v195 offset:256\n\tv_mfma_f32_16x16x32_bf16 v[20:23], v[172:175], a[220:223], v[20:23]\n\tbuffer_load_dwordx4 v202, s[20:23], s38 offen offset:128 lds\n\tv_mfma_f32_16x16x32_bf16 v[16:19], v[168:171], a[220:223], v[16:19]\n\tds_read_b64_tr_b16 v[156:157], v194 offset:5120\n\tv_mfma_f32_16x16x32_bf16 v[32:35], v[168:171], a[224:227], v[32:35]\n\tds_read_b64_tr_b16 v[158:159], v194 offset:5376\n\tv_mfma_f32_16x16x32_bf16 v[36:39], v[172:175], a[224:227], v[36:39]\n\ts_add_u32 m0, s39, 76608\n\tds_read_b64_tr_b16 v[160:161], v196 offset:4096\n\tv_mfma_f32_16x16x32_bf16 v[40:43], v[176:179], a[224:227], v[40:43]\n\tbuffer_load_dwordx4 v202, s[20:23], s38 offen offset:192 lds\n\tv_mfma_f32_16x16x32_bf16 v[44:47], v[180:183], a[224:227], v[44:47]\n\tds_read_b64_tr_b16 v[162:163], v196 offset:4352\n\tv_mfma_f32_16x16x32_bf16 v[60:63], v[180:183], a[228:231], v[60:63]\n\tds_read_b64_tr_b16 v[164:165], v197 offset:8192\n\tv_mfma_f32_16x16x32_bf16 v[56:59], v[176:179], a[228:231], v[56:59]\n\ts_add_u32 m0, s39, 77568\n\tds_read_b64_tr_b16 v[166:167], v197 offset:8448\n\tv_mfma_f32_16x16x32_bf16 v[52:55], v[172:175], a[228:231], v[52:55]\n\tbuffer_load_dwordx4 v202, s[20:23], s38 offen offset:256 lds\n\tv_mfma_f32_16x16x32_bf16 v[48:51], v[168:171], a[228:231], v[48:51]\n\tds_read_b128 a[248:251], v193 offset:0\n\tv_mfma_f32_16x16x32_bf16 v[136:139], v[184:187], a[252:255], v[136:139]\n\tds_read_b128 a[216:219], v192 offset:0\n\tv_mfma_f32_16x16x32_bf16 v[140:143], v[188:191], a[252:255], v[140:143]\n\ts_add_u32 m0, s39, 78528\n\tds_read_b128 a[252:255], v193 offset:18432\n\tv_mfma_f32_16x16x32_bf16 v[64:67], v[168:171], a[232:235], v[64:67]\n\tbuffer_load_dwordx4 v202, s[20:23], s38 offen offset:320 lds\n\tv_mfma_f32_16x16x32_bf16 v[68:71], v[172:175], a[232:235], v[68:71]\n\tds_read_b128 a[220:223], v192 offset:4608\n\tv_mfma_f32_16x16x32_bf16 v[72:75], v[176:179], a[232:235], v[72:75]\n\tds_read_b128 a[224:227], v192 offset:9216\n\tv_mfma_f32_16x16x32_bf16 v[76:79], v[180:183], a[232:235], v[76:79]\n\ts_add_u32 m0, s39, 79488\n\tds_read_b128 a[228:231], v192 offset:13824\n\tv_mfma_f32_16x16x32_bf16 v[92:95], v[180:183], a[236:239], v[92:95]\n\tbuffer_load_dwordx4 v202, s[20:23], s38 offen offset:384 lds\n\tv_mfma_f32_16x16x32_bf16 v[88:91], v[176:179], a[236:239], v[88:91]\n\tds_read_b128 a[232:235], v192 offset:18432\n\tv_mfma_f32_16x16x32_bf16 v[84:87], v[172:175], a[236:239], v[84:87]\n\tv_mfma_f32_16x16x32_bf16 v[80:83], v[168:171], a[236:239], v[80:83]\n\ts_waitcnt lgkmcnt(3)\n\ts_add_u32 m0, s39, 80448\n\tds_read_b128 a[236:239], v192 offset:23040\n\tv_mfma_f32_16x16x32_bf16 v[96:99], v[168:171], a[240:243], v[96:99]\n\tbuffer_load_dwordx4 v202, s[20:23], s38 offen offset:448 lds\n\tv_mfma_f32_16x16x32_bf16 v[100:103], v[172:175], a[240:243], v[100:103]\n\tv_mfma_f32_16x16x32_bf16 v[104:107], v[176:179], a[240:243], v[104:107]\n\tv_mfma_f32_16x16x32_bf16 v[108:111], v[180:183], a[240:243], v[108:111]\n\ts_add_u32 m0, s39, 81408\n\tds_read_b128 a[240:243], v192 offset:27648\n\tv_mfma_f32_16x16x32_bf16 v[124:127], v[180:183], a[244:247], v[124:127]\n\tbuffer_load_dwordx4 v202, s[20:23], s38 offen offset:512 lds\n\ts_add_u32 s38, s38, 36864\n\tv_mfma_f32_16x16x32_bf16 v[120:123], v[176:179], a[244:247], v[120:123]\n\tv_mfma_f32_16x16x32_bf16 v[116:119], v[172:175], a[244:247], v[116:119]\n\tv_mfma_f32_16x16x32_bf16 v[112:115], v[168:171], a[244:247], v[112:115]\n\ts_waitcnt vmcnt(9)\n\ts_barrier\n\ts_add_u32 m0, s47, 0\n\tds_read_b128 a[244:247], v192 offset:32256\n\tv_mfma_f32_16x16x32_bf16 v[128:131], v[160:163], a[248:251], v[128:131]\n\tds_read_b64_tr_b16 v[168:169], v194 offset:18432\n\tv_mfma_f32_16x16x32_bf16 v[132:135], v[164:167], a[248:251], v[132:135]\n\tds_read_b64_tr_b16 v[170:171], v194 offset:18688\n\tv_mfma_f32_16x16x32_bf16 v[0:3], v[144:147], a[216:219], v[0:3]\n\tds_read_b64_tr_b16 v[172:173], v195 offset:22528\n\tv_mfma_f32_16x16x32_bf16 v[4:7], v[148:151], a[216:219], v[4:7]\n\tds_read_b64_tr_b16 v[174:175], v195 offset:22784\n\tv_mfma_f32_16x16x32_bf16 v[8:11], v[152:155], a[216:219], v[8:11]\n\tds_read_b64_tr_b16 v[176:177], v195 offset:18432\n\tv_mfma_f32_16x16x32_bf16 v[12:15], v[156:159], a[216:219], v[12:15]\n\tds_read_b64_tr_b16 v[178:179], v195 offset:18688\n\tv_mfma_f32_16x16x32_bf16 v[28:31], v[156:159], a[220:223], v[28:31]\n\tds_read_b64_tr_b16 v[180:181], v194 offset:23552\n\tv_mfma_f32_16x16x32_bf16 v[24:27], v[152:155], a[220:223], v[24:27]\n\tds_read_b64_tr_b16 v[182:183], v194 offset:23808\n\tv_mfma_f32_16x16x32_bf16 v[20:23], v[148:151], a[220:223], v[20:23]\n\tds_read_b64_tr_b16 v[184:185], v196 offset:22528\n\tv_mfma_f32_16x16x32_bf16 v[16:19], v[144:147], a[220:223], v[16:19]\n\ts_waitcnt lgkmcnt(5)\n\tds_read_b64_tr_b16 v[186:187], v196 offset:22784\n\tv_mfma_f32_16x16x32_bf16 v[32:35], v[144:147], a[224:227], v[32:35]\n\tds_read_b64_tr_b16 v[188:189], v197 offset:26624\n\tv_mfma_f32_16x16x32_bf16 v[36:39], v[148:151], a[224:227], v[36:39]\n\tds_read_b64_tr_b16 v[190:191], v197 offset:26880\n\tv_mfma_f32_16x16x32_bf16 v[40:43], v[152:155], a[224:227], v[40:43]\n\tds_read_b64_tr_b16 a[168:169], v194 offset:36864\n\tv_mfma_f32_16x16x32_bf16 v[44:47], v[156:159], a[224:227], v[44:47]\n\tds_read_b64_tr_b16 a[170:171], v194 offset:37120\n\tv_mfma_f32_16x16x32_bf16 v[60:63], v[156:159], a[228:231], v[60:63]\n\tds_read_b64_tr_b16 a[172:173], v195 offset:40960\n\tv_mfma_f32_16x16x32_bf16 v[56:59], v[152:155], a[228:231], v[56:59]\n\tds_read_b64_tr_b16 a[174:175], v195 offset:41216\n\tv_mfma_f32_16x16x32_bf16 v[52:55], v[148:151], a[228:231], v[52:55]\n\tds_read_b64_tr_b16 a[176:177], v195 offset:36864\n\tv_mfma_f32_16x16x32_bf16 v[48:51], v[144:147], a[228:231], v[48:51]\n\tds_read_b64_tr_b16 a[178:179], v195 offset:37120\n\tv_mfma_f32_16x16x32_bf16 v[136:139], v[160:163], a[252:255], v[136:139]\n\tds_read_b64_tr_b16 a[180:181], v194 offset:41984\n\tv_mfma_f32_16x16x32_bf16 v[140:143], v[164:167], a[252:255], v[140:143]\n\tds_read_b64_tr_b16 a[182:183], v194 offset:42240\n\tv_mfma_f32_16x16x32_bf16 v[64:67], v[144:147], a[232:235], v[64:67]\n\tds_read_b64_tr_b16 a[184:185], v196 offset:40960\n\tv_mfma_f32_16x16x32_bf16 v[68:71], v[148:151], a[232:235], v[68:71]\n\tds_read_b64_tr_b16 a[186:187], v196 offset:41216\n\tv_mfma_f32_16x16x32_bf16 v[72:75], v[152:155], a[232:235], v[72:75]\n\tds_read_b64_tr_b16 a[188:189], v197 offset:45056\n\tv_mfma_f32_16x16x32_bf16 v[76:79], v[156:159], a[232:235], v[76:79]\n\tds_read_b64_tr_b16 a[190:191], v197 offset:45312\n\tv_mfma_f32_16x16x32_bf16 v[92:95], v[156:159], a[236:239], v[92:95]\n\tds_read_b64_tr_b16 a[192:193], v194 offset:55296\n\tv_mfma_f32_16x16x32_bf16 v[88:91], v[152:155], a[236:239], v[88:91]\n\tds_read_b64_tr_b16 a[194:195], v194 offset:55552\n\tv_mfma_f32_16x16x32_bf16 v[84:87], v[148:151], a[236:239], v[84:87]\n\tds_read_b64_tr_b16 a[196:197], v195 offset:59392\n\tv_mfma_f32_16x16x32_bf16 v[80:83], v[144:147], a[236:239], v[80:83]\n\tds_read_b64_tr_b16 a[198:199], v195 offset:59648\n\tv_mfma_f32_16x16x32_bf16 v[96:99], v[144:147], a[240:243], v[96:99]\n\tds_read_b64_tr_b16 a[200:201], v195 offset:55296\n\tv_mfma_f32_16x16x32_bf16 v[100:103], v[148:151], a[240:243], v[100:103]\n\tds_read_b64_tr_b16 a[202:203], v195 offset:55552\n\tv_mfma_f32_16x16x32_bf16 v[104:107], v[152:155], a[240:243], v[104:107]\n\tds_read_b64_tr_b16 a[204:205], v194 offset:60416\n\tv_mfma_f32_16x16x32_bf16 v[108:111], v[156:159], a[240:243], v[108:111]\n\tds_read_b64_tr_b16 a[206:207], v194 offset:60672\n\tv_mfma_f32_16x16x32_bf16 v[124:127], v[156:159], a[244:247], v[124:127]\n\tds_read_b64_tr_b16 a[208:209], v196 offset:59392\n\tv_mfma_f32_16x16x32_bf16 v[120:123], v[152:155], a[244:247], v[120:123]\n\tds_read_b64_tr_b16 a[210:211], v196 offset:59648\n\tv_mfma_f32_16x16x32_bf16 v[116:119], v[148:151], a[244:247], v[116:119]\n\tds_read_b64_tr_b16 a[212:213], v197 offset:63488\n\tv_mfma_f32_16x16x32_bf16 v[112:115], v[144:147], a[244:247], v[112:115]\n\tds_read_b64_tr_b16 a[214:215], v197 offset:63744\n\tds_read_b128 a[216:219], v193 offset:20736\n\tds_read_b128 a[220:223], v193 offset:20800\n\tds_read_b128 a[224:227], v193 offset:20864\n\tds_read_b128 a[228:231], v193 offset:20928\n\tds_read_b128 a[232:235], v204 offset:20992\n\tds_read_b128 a[236:239], v193 offset:20192\n\ts_waitcnt lgkmcnt(5)\n\tv_mfma_f32_16x16x32_bf16 v[136:139], a[16:19], a[216:219], v[136:139]\n\tds_read_b128 a[240:243], v193 offset:20256\n\tv_mfma_f32_16x16x32_bf16 v[140:143], a[20:23], a[216:219], v[140:143]\n\ts_waitcnt lgkmcnt(4)\n\tds_read_b128 a[244:247], v193 offset:64\n\tv_mfma_f32_16x16x32_bf16 v[136:139], a[40:43], a[220:223], v[136:139]\n\tds_read_b128 a[248:251], v193 offset:128\n\tv_mfma_f32_16x16x32_bf16 v[140:143], a[44:47], a[220:223], v[140:143]\n\tds_read_b128 a[252:255], v193 offset:192\n\tv_mfma_f32_16x16x32_bf16 v[136:139], a[64:67], a[224:227], v[136:139]\n\tds_read_b64 a[216:217], v206 offset:256\n\tv_mfma_f32_16x16x32_bf16 v[140:143], a[68:71], a[224:227], v[140:143]\n\ts_waitcnt lgkmcnt(5)\n\tds_read_b128 a[220:223], v192 offset:64\n\tv_mfma_f32_16x16x32_bf16 v[136:139], a[88:91], a[228:231], v[136:139]\n\tds_read_b128 a[224:227], v192 offset:128\n\tv_mfma_f32_16x16x32_bf16 v[140:143], a[92:95], a[228:231], v[140:143]\n\tds_read_b128 a[228:231], v192 offset:192\n\tv_mfma_f32_16x16x32_bf16 v[136:139], a[112:115], a[232:235], v[136:139]\n\tv_mfma_f32_16x16x32_bf16 v[140:143], a[116:119], a[232:235], v[140:143]\n\tds_read_b64 a[232:233], v205 offset:256\n\tv_mfma_f32_16x16x32_bf16 v[136:139], a[136:139], a[236:239], v[136:139]\n\ts_waitcnt vmcnt(0)\n\ts_barrier\n\tv_mfma_f32_16x16x32_bf16 v[140:143], a[140:143], a[236:239], v[140:143]\n\ts_waitcnt lgkmcnt(3)\n\tbuffer_load_dwordx4 v214, s[24:27], s41 offen lds\n\ts_mov_b32 s41, 4096\n\tv_mfma_f32_16x16x32_bf16 v[136:139], a[160:163], a[240:243], v[136:139]\n\tds_read_b64_tr_b16 v[144:145], v207 offset:0\n\tv_mfma_f32_16x16x32_bf16 v[140:143], a[164:167], a[240:243], v[140:143]\n\tds_read_b64_tr_b16 v[148:149], v208 offset:4096\n\tv_mfma_f32_16x16x32_bf16 v[128:131], v[184:187], a[244:247], v[128:131]\n\ts_add_u32 m0, s47, 4096\n\tds_read_b64_tr_b16 v[152:153], v208 offset:0\n\tv_mfma_f32_16x16x32_bf16 v[132:135], v[188:191], a[244:247], v[132:135]\n\tbuffer_load_dwordx4 v214, s[24:27], s41 offen lds\n\ts_mov_b32 s41, 8192\n\tv_mfma_f32_16x16x32_bf16 v[128:131], a[184:187], a[248:251], v[128:131]\n\tds_read_b64_tr_b16 v[156:157], v207 offset:5120\n\tv_mfma_f32_16x16x32_bf16 v[132:135], a[188:191], a[248:251], v[132:135]\n\tds_read_b64_tr_b16 v[160:161], v209 offset:4096\n\tv_mfma_f32_16x16x32_bf16 v[128:131], a[208:211], a[252:255], v[128:131]\n\ts_add_u32 m0, s47, 8192\n\tds_read_b64_tr_b16 v[164:165], v210 offset:8192\n\tv_mfma_f32_16x16x32_bf16 v[132:135], a[212:215], a[252:255], v[132:135]\n\tbuffer_load_dwordx4 v214, s[24:27], s41 offen lds\n\ts_mov_b32 s41, 12288\n\tv_mfma_f32_16x16x32_bf16 v[0:3], v[168:171], a[220:223], v[0:3]\n\tds_read_b128 a[236:239], v192 offset:4672\n\tv_mfma_f32_16x16x32_bf16 v[4:7], v[172:175], a[220:223], v[4:7]\n\tds_read_b128 a[240:243], v192 offset:4736\n\tv_mfma_f32_16x16x32_bf16 v[8:11], v[176:179], a[220:223], v[8:11]\n\ts_add_u32 m0, s47, 12288\n\tds_read_b128 a[244:247], v192 offset:4800\n\tv_mfma_f32_16x16x32_bf16 v[12:15], v[180:183], a[220:223], v[12:15]\n\ts_waitcnt lgkmcnt(4)\n\tbuffer_load_dwordx4 v214, s[24:27], s41 offen lds\n\ts_mov_b32 s41, 16384\n\tv_mfma_f32_16x16x16_bf16 v[128:131], v[160:161], a[216:217], v[128:131]\n\ts_waitcnt lgkmcnt(3)\n\tds_read_b64 a[248:249], v205 offset:4864\n\tv_mfma_f32_16x16x16_bf16 v[132:135], v[164:165], a[216:217], v[132:135]\n\tds_read_b128 a[252:255], v192 offset:9280\n\tv_mfma_f32_16x16x32_bf16 v[12:15], a[180:183], a[224:227], v[12:15]\n\ts_add_u32 m0, s47, 16384\n\tds_read_b128 a[216:219], v192 offset:9344\n\tv_mfma_f32_16x16x32_bf16 v[8:11], a[176:179], a[224:227], v[8:11]\n\tbuffer_load_dwordx4 v214, s[24:27], s41 offen lds\n\ts_mov_b32 s41, 20480\n\tv_mfma_f32_16x16x32_bf16 v[4:7], a[172:175], a[224:227], v[4:7]\n\tds_read_b128 a[220:223], v192 offset:9408\n\tv_mul_f32 v236, s42, v128\n\tv_mfma_f32_16x16x32_bf16 v[0:3], a[168:171], a[224:227], v[0:3]\n\tds_read_b64 a[224:225], v205 offset:9472\n\tv_mul_f32 v237, s42, v129\n\tv_mfma_f32_16x16x32_bf16 v[0:3], a[192:195], a[228:231], v[0:3]\n\ts_add_u32 m0, s47, 20480\n\tv_mul_f32 v238, s42, v130\n\tv_mfma_f32_16x16x32_bf16 v[4:7], a[196:199], a[228:231], v[4:7]\n\tbuffer_load_dwordx4 v214, s[24:27], s41 offen lds\n\ts_mov_b32 s41, 24576\n\tv_mfma_f32_16x16x32_bf16 v[8:11], a[200:203], a[228:231], v[8:11]\n\tv_mul_f32 v239, s42, v131\n\tv_mul_f32 v132, s43, v132\n\tv_mfma_f32_16x16x32_bf16 v[12:15], a[204:207], a[228:231], v[12:15]\n\tds_read_b128 a[228:231], v192 offset:13888\n\tv_mul_f32 v133, s43, v133\n\tv_mfma_f32_16x16x16_bf16 v[4:7], v[148:149], a[232:233], v[4:7]\n\ts_add_u32 m0, s47, 24576\n\tv_mul_f32 v134, s43, v134\n\tv_mfma_f32_16x16x16_bf16 v[8:11], v[152:153], a[232:233], v[8:11]\n\tbuffer_load_dwordx4 v214, s[24:27], s41 offen lds\n\ts_mov_b32 s41, 28672\n\tv_mfma_f32_16x16x16_bf16 v[12:15], v[156:157], a[232:233], v[12:15]\n\tv_mul_f32 v135, s43, v135\n\tv_exp_f32_e64 v236, -|v236|\n\tv_mfma_f32_16x16x16_bf16 v[0:3], v[144:145], a[232:233], v[0:3]\n\ts_waitcnt lgkmcnt(1)\n\tds_read_b128 a[232:235], v192 offset:13952\n\tv_add_f32 v244, 1.0, v236\n\tv_mfma_f32_16x16x32_bf16 v[16:19], v[168:171], a[236:239], v[16:19]\n\ts_add_u32 m0, s47, 28672\n\tv_exp_f32_e64 v237, -|v237|\n\tv_mfma_f32_16x16x32_bf16 v[20:23], v[172:175], a[236:239], v[20:23]\n\tbuffer_load_dwordx4 v214, s[24:27], s41 offen lds\n\ts_mov_b32 s41, 32768\n\tv_mfma_f32_16x16x32_bf16 v[24:27], v[176:179], a[236:239], v[24:27]\n\tv_exp_f32_e64 v238, -|v238|\n\tv_mfma_f32_16x16x32_bf16 v[28:31], v[180:183], a[236:239], v[28:31]\n\tds_read_b128 a[236:239], v192 offset:14016\n\tv_add_f32 v245, 1.0, v237\n\tv_mfma_f32_16x16x32_bf16 v[28:31], a[180:183], a[240:243], v[28:31]\n\ts_add_u32 m0, s47, 32768\n\tv_add_f32 v246, 1.0, v238\n\tv_mfma_f32_16x16x32_bf16 v[24:27], a[176:179], a[240:243], v[24:27]\n\tbuffer_load_dwordx4 v214, s[24:27], s41 offen lds\n\ts_mov_b32 s41, 36864\n\tv_mfma_f32_16x16x32_bf16 v[20:23], a[172:175], a[240:243], v[20:23]\n\tv_exp_f32_e64 v239, -|v239|\n\tv_mfma_f32_16x16x32_bf16 v[16:19], a[168:171], a[240:243], v[16:19]\n\tds_read_b64 a[240:241], v205 offset:14080\n\tv_add_f32 v247, 1.0, v239\n\tv_mfma_f32_16x16x32_bf16 v[16:19], a[192:195], a[244:247], v[16:19]\n\ts_add_u32 m0, s47, 36864\n\tv_mul_f32 v240, s42, v0\n\tv_mfma_f32_16x16x32_bf16 v[20:23], a[196:199], a[244:247], v[20:23]\n\tbuffer_load_dwordx4 v214, s[24:27], s41 offen lds\n\ts_mov_b32 s41, 40960\n\tv_mfma_f32_16x16x32_bf16 v[24:27], a[200:203], a[244:247], v[24:27]\n\tv_exp_f32 v132, v132\n\tv_mfma_f32_16x16x32_bf16 v[28:31], a[204:207], a[244:247], v[28:31]\n\tds_read_b128 a[244:247], v193 offset:18496\n\tv_fmac_f32 v244, v244, v132\n\tv_mfma_f32_16x16x16_bf16 v[20:23], v[148:149], a[248:249], v[20:23]\n\ts_add_u32 m0, s47, 40960\n\tv_mul_f32 v241, s42, v1\n\tv_mfma_f32_16x16x16_bf16 v[24:27], v[152:153], a[248:249], v[24:27]\n\tbuffer_load_dwordx4 v214, s[24:27], s41 offen lds\n\ts_mov_b32 s41, 0\n\tv_mfma_f32_16x16x16_bf16 v[28:31], v[156:157], a[248:249], v[28:31]\n\tv_exp_f32 v133, v133\n\tv_mfma_f32_16x16x16_bf16 v[16:19], v[144:145], a[248:249], v[16:19]\n\tds_read_b128 a[248:251], v193 offset:18560\n\tv_fmac_f32 v245, v245, v133\n\tv_mfma_f32_16x16x32_bf16 v[32:35], v[168:171], a[252:255], v[32:35]\n\tv_exp_f32 v134, v134\n\tv_mfma_f32_16x16x32_bf16 v[36:39], v[172:175], a[252:255], v[36:39]\n\tv_exp_f32 v135, v135\n\tv_mfma_f32_16x16x32_bf16 v[40:43], v[176:179], a[252:255], v[40:43]\n\tv_fmac_f32 v246, v246, v134\n\tv_fmac_f32 v247, v247, v135\n\tv_mfma_f32_16x16x32_bf16 v[44:47], v[180:183], a[252:255], v[44:47]\n\tds_read_b128 a[252:255], v193 offset:18624\n\tv_mul_f32 v242, s42, v2\n\tv_mfma_f32_16x16x32_bf16 v[44:47], a[180:183], a[216:219], v[44:47]\n\tv_rcp_f32 v132, v244\n\tv_mfma_f32_16x16x32_bf16 v[40:43], a[176:179], a[216:219], v[40:43]\n\tv_rcp_f32 v133, v245\n\tv_mfma_f32_16x16x32_bf16 v[36:39], a[172:175], a[216:219], v[36:39]\n\tv_rcp_f32 v134, v246\n\tv_mfma_f32_16x16x32_bf16 v[32:35], a[168:171], a[216:219], v[32:35]\n\tds_read_b64 a[216:217], v206 offset:18688\n\tv_fma_f32 v132, -v236, v132, v132\n\tv_mfma_f32_16x16x32_bf16 v[32:35], a[192:195], a[220:223], v[32:35]\n\tv_rcp_f32 v135, v247\n\tv_mfma_f32_16x16x32_bf16 v[36:39], a[196:199], a[220:223], v[36:39]\n\tv_fma_f32 v133, -v237, v133, v133\n\tv_fma_f32 v134, -v238, v134, v134\n\tv_mfma_f32_16x16x32_bf16 v[40:43], a[200:203], a[220:223], v[40:43]\n\tv_fma_f32 v135, -v239, v135, v135\n\tv_bfi_b32 v132, s44, v132, v128\n\tv_mfma_f32_16x16x32_bf16 v[44:47], a[204:207], a[220:223], v[44:47]\n\tds_read_b128 a[220:223], v192 offset:18496\n\tv_bfi_b32 v133, s44, v133, v129\n\tv_mfma_f32_16x16x16_bf16 v[36:39], v[148:149], a[224:225], v[36:39]\n\tv_bfi_b32 v134, s44, v134, v130\n\tv_bfi_b32 v135, s44, v135, v131\n\tv_mfma_f32_16x16x16_bf16 v[40:43], v[152:153], a[224:225], v[40:43]\n\tv_cvt_pk_bf16_f32 v128, v132, v133\n\tv_cvt_pk_bf16_f32 v129, v134, v135\n\tv_mfma_f32_16x16x16_bf16 v[44:47], v[156:157], a[224:225], v[44:47]\n\tds_write_b64 v216, v[128:129] offset:49152\n\tv_mul_f32 v243, s42, v3\n\tv_mul_f32 v4, s43, v4\n\tv_mfma_f32_16x16x16_bf16 v[32:35], v[144:145], a[224:225], v[32:35]\n\ts_waitcnt lgkmcnt(2)\n\tv_mul_f32 v5, s43, v5\n\tds_read_b128 a[224:227], v192 offset:18560\n\tv_mfma_f32_16x16x32_bf16 v[48:51], v[168:171], a[228:231], v[48:51]\n\tv_mul_f32 v6, s43, v6\n\tv_mul_f32 v7, s43, v7\n\tv_mfma_f32_16x16x32_bf16 v[52:55], v[172:175], a[228:231], v[52:55]\n\tv_exp_f32_e64 v240, -|v240|\n\tv_mfma_f32_16x16x32_bf16 v[56:59], v[176:179], a[228:231], v[56:59]\n\tv_exp_f32_e64 v241, -|v241|\n\tv_mfma_f32_16x16x32_bf16 v[60:63], v[180:183], a[228:231], v[60:63]\n\tds_read_b128 a[228:231], v192 offset:18624\n\tv_add_f32 v244, 1.0, v240\n\tv_mfma_f32_16x16x32_bf16 v[60:63], a[180:183], a[232:235], v[60:63]\n\tv_exp_f32_e64 v242, -|v242|\n\tv_mfma_f32_16x16x32_bf16 v[56:59], a[176:179], a[232:235], v[56:59]\n\tv_exp_f32_e64 v243, -|v243|\n\tv_mfma_f32_16x16x32_bf16 v[52:55], a[172:175], a[232:235], v[52:55]\n\tv_exp_f32 v4, v4\n\tv_mfma_f32_16x16x32_bf16 v[48:51], a[168:171], a[232:235], v[48:51]\n\tds_read_b64 a[232:233], v205 offset:18688\n\tv_add_f32 v245, 1.0, v241\n\tv_mfma_f32_16x16x32_bf16 v[48:51], a[192:195], a[236:239], v[48:51]\n\tv_exp_f32 v5, v5\n\tv_mfma_f32_16x16x32_bf16 v[52:55], a[196:199], a[236:239], v[52:55]\n\tv_exp_f32 v6, v6\n\tv_mfma_f32_16x16x32_bf16 v[56:59], a[200:203], a[236:239], v[56:59]\n\tv_exp_f32 v7, v7\n\tv_mfma_f32_16x16x32_bf16 v[60:63], a[204:207], a[236:239], v[60:63]\n\tds_read_b128 a[236:239], v192 offset:23104\n\tv_add_f32 v246, 1.0, v242\n\tv_mfma_f32_16x16x16_bf16 v[52:55], v[148:149], a[240:241], v[52:55]\n\tv_add_f32 v247, 1.0, v243\n\tv_fmac_f32 v244, v244, v4\n\tv_mfma_f32_16x16x16_bf16 v[56:59], v[152:153], a[240:241], v[56:59]\n\tv_fmac_f32 v245, v245, v5\n\tv_fmac_f32 v246, v246, v6\n\tv_mfma_f32_16x16x16_bf16 v[60:63], v[156:157], a[240:241], v[60:63]\n\tv_fmac_f32 v247, v247, v7\n\tv_mul_f32 v236, s42, v8\n\tv_mfma_f32_16x16x16_bf16 v[48:51], v[144:145], a[240:241], v[48:51]\n\tds_read_b128 a[240:243], v192 offset:23168\n\tv_mul_f32 v237, s42, v9\n\tv_mfma_f32_16x16x32_bf16 v[136:139], v[184:187], a[244:247], v[136:139]\n\tv_rcp_f32 v4, v244\n\tv_mfma_f32_16x16x32_bf16 v[140:143], v[188:191], a[244:247], v[140:143]\n\tds_read_b128 a[244:247], v192 offset:23232\n\tv_fma_f32 v4, -v240, v4, v4\n\tv_mfma_f32_16x16x32_bf16 v[136:139], a[184:187], a[248:251], v[136:139]\n\tv_rcp_f32 v5, v245\n\tv_mfma_f32_16x16x32_bf16 v[140:143], a[188:191], a[248:251], v[140:143]\n\tds_read_b64 a[248:249], v205 offset:23296\n\tv_fma_f32 v5, -v241, v5, v5\n\tv_mfma_f32_16x16x32_bf16 v[136:139], a[208:211], a[252:255], v[136:139]\n\tv_rcp_f32 v6, v246\n\tv_mfma_f32_16x16x32_bf16 v[140:143], a[212:215], a[252:255], v[140:143]\n\ts_waitcnt lgkmcnt(2)\n\tds_read_b128 a[252:255], v192 offset:27712\n\tv_fma_f32 v6, -v242, v6, v6\n\tv_mfma_f32_16x16x32_bf16 v[64:67], v[168:171], a[220:223], v[64:67]\n\tv_rcp_f32 v7, v247\n\tv_mfma_f32_16x16x16_bf16 v[136:139], v[160:161], a[216:217], v[136:139]\n\tv_fma_f32 v7, -v243, v7, v7\n\tv_bfi_b32 v4, s44, v4, v0\n\tv_mfma_f32_16x16x16_bf16 v[140:143], v[164:165], a[216:217], v[140:143]\n\tds_read_b128 a[216:219], v192 offset:27776\n\tv_bfi_b32 v5, s44, v5, v1\n\tv_mfma_f32_16x16x32_bf16 v[68:71], v[172:175], a[220:223], v[68:71]\n\tv_bfi_b32 v6, s44, v6, v2\n\tv_bfi_b32 v7, s44, v7, v3\n\tv_mfma_f32_16x16x32_bf16 v[72:75], v[176:179], a[220:223], v[72:75]\n\tv_mul_f32 v238, s42, v10\n\tv_mul_f32 v239, s42, v11\n\tv_mfma_f32_16x16x32_bf16 v[76:79], v[180:183], a[220:223], v[76:79]\n\tds_read_b128 a[220:223], v192 offset:27840\n\tv_mul_f32 v12, s43, v12\n\tv_mfma_f32_16x16x32_bf16 v[76:79], a[180:183], a[224:227], v[76:79]\n\tv_mul_f32 v13, s43, v13\n\tv_mul_f32 v14, s43, v14\n\tv_mfma_f32_16x16x32_bf16 v[72:75], a[176:179], a[224:227], v[72:75]\n\tv_mul_f32 v15, s43, v15\n\tv_cvt_pk_bf16_f32 v0, v4, v5\n\tv_mfma_f32_16x16x32_bf16 v[68:71], a[172:175], a[224:227], v[68:71]\n\tv_exp_f32_e64 v236, -|v236|\n\tv_mfma_f32_16x16x32_bf16 v[64:67], a[168:171], a[224:227], v[64:67]\n\tds_read_b64 a[224:225], v205 offset:27904\n\tv_add_f32 v244, 1.0, v236\n\tv_mfma_f32_16x16x32_bf16 v[64:67], a[192:195], a[228:231], v[64:67]\n\tv_exp_f32_e64 v237, -|v237|\n\tv_mfma_f32_16x16x32_bf16 v[68:71], a[196:199], a[228:231], v[68:71]\n\tv_exp_f32_e64 v238, -|v238|\n\tv_mfma_f32_16x16x32_bf16 v[72:75], a[200:203], a[228:231], v[72:75]\n\tv_exp_f32_e64 v239, -|v239|\n\tv_mfma_f32_16x16x32_bf16 v[76:79], a[204:207], a[228:231], v[76:79]\n\tds_read_b128 a[228:231], v192 offset:32320\n\tv_add_f32 v245, 1.0, v237\n\tv_mfma_f32_16x16x16_bf16 v[68:71], v[148:149], a[232:233], v[68:71]\n\tv_exp_f32 v12, v12\n\tv_mfma_f32_16x16x16_bf16 v[72:75], v[152:153], a[232:233], v[72:75]\n\tv_exp_f32 v13, v13\n\tv_mfma_f32_16x16x16_bf16 v[76:79], v[156:157], a[232:233], v[76:79]\n\ts_waitcnt vmcnt(0)\n\ts_barrier\n\tv_exp_f32 v14, v14\n\tv_mfma_f32_16x16x16_bf16 v[64:67], v[144:145], a[232:233], v[64:67]\n\tds_read_b64_tr_b16 a[16:17], v217 offset:0\n\tv_add_f32 v246, 1.0, v238\n\tv_mfma_f32_16x16x32_bf16 v[80:83], v[168:171], a[236:239], v[80:83]\n\tds_read_b64_tr_b16 a[18:19], v217 offset:4608\n\tv_add_f32 v247, 1.0, v239\n\tv_mfma_f32_16x16x32_bf16 v[84:87], v[172:175], a[236:239], v[84:87]\n\tds_read_b64_tr_b16 a[20:21], v217 offset:9216\n\tv_fmac_f32 v244, v244, v12\n\tv_mfma_f32_16x16x32_bf16 v[88:91], v[176:179], a[236:239], v[88:91]\n\tds_read_b64_tr_b16 a[22:23], v217 offset:13824\n\tv_fmac_f32 v245, v245, v13\n\tv_mfma_f32_16x16x32_bf16 v[92:95], v[180:183], a[236:239], v[92:95]\n\tds_read_b64_tr_b16 a[40:41], v217 offset:18432\n\tv_fmac_f32 v246, v246, v14\n\tv_mfma_f32_16x16x32_bf16 v[92:95], a[180:183], a[240:243], v[92:95]\n\tds_read_b64_tr_b16 a[42:43], v217 offset:23040\n\tv_cvt_pk_bf16_f32 v1, v6, v7\n\tv_mfma_f32_16x16x32_bf16 v[88:91], a[176:179], a[240:243], v[88:91]\n\tds_read_b64_tr_b16 a[44:45], v217 offset:27648\n\tv_mul_f32 v240, s42, v16\n\tv_mfma_f32_16x16x32_bf16 v[84:87], a[172:175], a[240:243], v[84:87]\n\tds_read_b64_tr_b16 a[46:47], v217 offset:32256\n\tv_mul_f32 v241, s42, v17\n\tv_mfma_f32_16x16x32_bf16 v[80:83], a[168:171], a[240:243], v[80:83]\n\ts_waitcnt lgkmcnt(5)\n\tds_read_b64_tr_b16 v[250:251], v217 offset:36864\n\tv_mul_f32 v242, s42, v18\n\tv_mfma_f32_16x16x32_bf16 v[80:83], a[192:195], a[244:247], v[80:83]\n\tds_read_b64_tr_b16 a[64:65], v217 offset:8\n\tv_mul_f32 v243, s42, v19\n\tv_mfma_f32_16x16x32_bf16 v[84:87], a[196:199], a[244:247], v[84:87]\n\tds_read_b64_tr_b16 a[66:67], v217 offset:4616\n\tv_mul_f32 v20, s43, v20\n\tv_mfma_f32_16x16x32_bf16 v[88:91], a[200:203], a[244:247], v[88:91]\n\tds_read_b64_tr_b16 a[68:69], v217 offset:9224\n\tv_mul_f32 v21, s43, v21\n\tv_mfma_f32_16x16x32_bf16 v[92:95], a[204:207], a[244:247], v[92:95]\n\tds_read_b64_tr_b16 a[70:71], v217 offset:13832\n\tv_mul_f32 v22, s43, v22\n\tv_mfma_f32_16x16x16_bf16 v[84:87], v[148:149], a[248:249], v[84:87]\n\tds_read_b64_tr_b16 a[88:89], v217 offset:18440\n\tv_mul_f32 v23, s43, v23\n\tv_mfma_f32_16x16x16_bf16 v[88:91], v[152:153], a[248:249], v[88:91]\n\tds_read_b64_tr_b16 a[90:91], v217 offset:23048\n\tv_mul_f32 v28, s43, v28\n\tv_mfma_f32_16x16x16_bf16 v[92:95], v[156:157], a[248:249], v[92:95]\n\tds_read_b64_tr_b16 a[92:93], v217 offset:27656\n\tv_mul_f32 v29, s43, v29\n\tv_mfma_f32_16x16x16_bf16 v[80:83], v[144:145], a[248:249], v[80:83]\n\tds_read_b64_tr_b16 a[94:95], v217 offset:32264\n\tv_mul_f32 v30, s43, v30\n\tv_mfma_f32_16x16x32_bf16 v[96:99], v[168:171], a[252:255], v[96:99]\n\tds_read_b64_tr_b16 v[252:253], v217 offset:36872\n\tv_mul_f32 v31, s43, v31\n\tv_mfma_f32_16x16x32_bf16 v[100:103], v[172:175], a[252:255], v[100:103]\n\tds_read_b64_tr_b16 a[112:113], v218 offset:256\n\tv_mul_f32 v36, s43, v36\n\tv_mfma_f32_16x16x32_bf16 v[104:107], v[176:179], a[252:255], v[104:107]\n\tds_read_b64_tr_b16 a[114:115], v218 offset:4864\n\tv_mul_f32 v37, s43, v37\n\tv_mfma_f32_16x16x32_bf16 v[108:111], v[180:183], a[252:255], v[108:111]\n\tds_read_b64_tr_b16 a[116:117], v218 offset:9472\n\tv_mul_f32 v38, s43, v38\n\tv_mfma_f32_16x16x32_bf16 v[108:111], a[180:183], a[216:219], v[108:111]\n\tds_read_b64_tr_b16 a[118:119], v218 offset:14080\n\tv_mul_f32 v39, s43, v39\n\tv_mfma_f32_16x16x32_bf16 v[104:107], a[176:179], a[216:219], v[104:107]\n\tds_read_b64_tr_b16 a[136:137], v218 offset:18688\n\tv_mul_f32 v44, s43, v44\n\tv_mfma_f32_16x16x32_bf16 v[100:103], a[172:175], a[216:219], v[100:103]\n\tds_read_b64_tr_b16 a[138:139], v218 offset:23296\n\tv_mul_f32 v45, s43, v45\n\tv_mfma_f32_16x16x32_bf16 v[96:99], a[168:171], a[216:219], v[96:99]\n\tds_read_b64_tr_b16 a[140:141], v218 offset:27904\n\tv_mul_f32 v46, s43, v46\n\tv_mfma_f32_16x16x32_bf16 v[96:99], a[192:195], a[220:223], v[96:99]\n\tds_read_b64_tr_b16 a[142:143], v218 offset:32512\n\tv_mul_f32 v47, s43, v47\n\tv_mfma_f32_16x16x32_bf16 v[100:103], a[196:199], a[220:223], v[100:103]\n\tds_read_b64_tr_b16 v[254:255], v218 offset:37120\n\tv_mul_f32 v52, s43, v52\n\tv_mfma_f32_16x16x32_bf16 v[104:107], a[200:203], a[220:223], v[104:107]\n\tds_read_b128 a[232:235], v192 offset:32384\n\tv_mul_f32 v53, s43, v53\n\tv_mfma_f32_16x16x32_bf16 v[108:111], a[204:207], a[220:223], v[108:111]\n\tds_read_b128 a[236:239], v192 offset:32448\n\tv_mul_f32 v54, s43, v54\n\tv_mfma_f32_16x16x16_bf16 v[100:103], v[148:149], a[224:225], v[100:103]\n\tds_read_b64 a[240:241], v205 offset:32512\n\tv_mul_f32 v55, s43, v55\n\tv_mfma_f32_16x16x16_bf16 v[104:107], v[152:153], a[224:225], v[104:107]\n\tds_read_b128 a[244:247], v192 offset:29952\n\tv_mul_f32 v60, s43, v60\n\tv_mfma_f32_16x16x16_bf16 v[108:111], v[156:157], a[224:225], v[108:111]\n\tds_read_b128 a[248:251], v192 offset:30016\n\tv_mul_f32 v61, s43, v61\n\tv_mfma_f32_16x16x16_bf16 v[96:99], v[144:145], a[224:225], v[96:99]\n\tds_read_b128 a[252:255], v192 offset:30080\n\tv_mul_f32 v62, s43, v62\n\tv_mfma_f32_16x16x32_bf16 v[112:115], v[168:171], a[228:231], v[112:115]\n\tds_read_b128 a[216:219], v192 offset:30144\n\tv_mul_f32 v63, s43, v63\n\tv_mfma_f32_16x16x32_bf16 v[116:119], v[172:175], a[228:231], v[116:119]\n\tds_read_b128 a[220:223], v203 offset:30208\n\tv_mul_f32 v140, s43, v140\n\tv_mfma_f32_16x16x32_bf16 v[120:123], v[176:179], a[228:231], v[120:123]\n\tds_read_b128 a[224:227], v192 offset:29408\n\tv_mul_f32 v141, s43, v141\n\tv_mfma_f32_16x16x32_bf16 v[124:127], v[180:183], a[228:231], v[124:127]\n\ts_waitcnt lgkmcnt(5)\n\tds_read_b128 a[228:231], v192 offset:29472\n\tv_mul_f32 v142, s43, v142\n\tv_mfma_f32_16x16x32_bf16 v[124:127], a[180:183], a[232:235], v[124:127]\n\tv_exp_f32 v15, v15\n\tv_mfma_f32_16x16x32_bf16 v[120:123], a[176:179], a[232:235], v[120:123]\n\tv_fmac_f32 v247, v247, v15\n\tv_mul_f32 v143, s43, v143\n\tv_mfma_f32_16x16x32_bf16 v[116:119], a[172:175], a[232:235], v[116:119]\n\tv_rcp_f32 v12, v244\n\tv_mfma_f32_16x16x32_bf16 v[112:115], a[168:171], a[232:235], v[112:115]\n\tds_read_b128 a[232:235], v192 offset:34560\n\tv_fma_f32 v12, -v236, v12, v12\n\tv_mfma_f32_16x16x32_bf16 v[112:115], a[192:195], a[236:239], v[112:115]\n\tv_rcp_f32 v13, v245\n\tv_mfma_f32_16x16x32_bf16 v[116:119], a[196:199], a[236:239], v[116:119]\n\tv_rcp_f32 v14, v246\n\tv_mfma_f32_16x16x32_bf16 v[120:123], a[200:203], a[236:239], v[120:123]\n\tv_rcp_f32 v15, v247\n\tv_mfma_f32_16x16x32_bf16 v[124:127], a[204:207], a[236:239], v[124:127]\n\tds_read_b128 a[236:239], v192 offset:34624\n\tv_fma_f32 v13, -v237, v13, v13\n\tv_mfma_f32_16x16x16_bf16 v[116:119], v[148:149], a[240:241], v[116:119]\n\tv_fma_f32 v14, -v238, v14, v14\n\tv_fma_f32 v15, -v239, v15, v15\n\tv_mfma_f32_16x16x16_bf16 v[120:123], v[152:153], a[240:241], v[120:123]\n\tv_bfi_b32 v12, s44, v12, v8\n\tv_bfi_b32 v13, s44, v13, v9\n\tv_mfma_f32_16x16x16_bf16 v[124:127], v[156:157], a[240:241], v[124:127]\n\tv_bfi_b32 v14, s44, v14, v10\n\tv_bfi_b32 v15, s44, v15, v11\n\tv_mfma_f32_16x16x16_bf16 v[112:115], v[144:145], a[240:241], v[112:115]\n\tds_read_b128 a[240:243], v192 offset:34688\n\tv_cvt_pk_bf16_f32 v2, v12, v13\n\tv_mfma_f32_16x16x32_bf16 v[96:99], a[0:3], a[244:247], v[96:99]\n\tv_cvt_pk_bf16_f32 v3, v14, v15\n\tv_mul_f32 v236, s42, v24\n\tv_mfma_f32_16x16x32_bf16 v[100:103], a[4:7], a[244:247], v[100:103]\n\tds_write_b128 v214, v[0:3] offset:45056\n\tv_mul_f32 v237, s42, v25\n\tv_mfma_f32_16x16x32_bf16 v[104:107], a[8:11], a[244:247], v[104:107]\n\tv_exp_f32_e64 v240, -|v240|\n\tv_mfma_f32_16x16x32_bf16 v[108:111], a[12:15], a[244:247], v[108:111]\n\ts_waitcnt lgkmcnt(2)\n\tds_read_b128 a[244:247], v192 offset:34752\n\tv_add_f32 v244, 1.0, v240\n\tv_mfma_f32_16x16x32_bf16 v[96:99], a[24:27], a[248:251], v[96:99]\n\tv_exp_f32_e64 v241, -|v241|\n\tv_mfma_f32_16x16x32_bf16 v[100:103], a[28:31], a[248:251], v[100:103]\n\tv_exp_f32_e64 v242, -|v242|\n\tv_mfma_f32_16x16x32_bf16 v[104:107], a[32:35], a[248:251], v[104:107]\n\tv_exp_f32_e64 v243, -|v243|\n\tv_mfma_f32_16x16x32_bf16 v[108:111], a[36:39], a[248:251], v[108:111]\n\tds_read_b128 a[248:251], v203 offset:34816\n\tv_add_f32 v245, 1.0, v241\n\tv_mfma_f32_16x16x32_bf16 v[96:99], a[48:51], a[252:255], v[96:99]\n\tv_exp_f32 v20, v20\n\tv_mfma_f32_16x16x32_bf16 v[100:103], a[52:55], a[252:255], v[100:103]\n\tv_exp_f32 v21, v21\n\tv_mfma_f32_16x16x32_bf16 v[104:107], a[56:59], a[252:255], v[104:107]\n\tv_exp_f32 v22, v22\n\tv_mfma_f32_16x16x32_bf16 v[108:111], a[60:63], a[252:255], v[108:111]\n\tds_read_b128 a[252:255], v192 offset:34016\n\tv_add_f32 v246, 1.0, v242\n\tv_mfma_f32_16x16x32_bf16 v[96:99], a[72:75], a[216:219], v[96:99]\n\tv_exp_f32 v23, v23\n\tv_mfma_f32_16x16x32_bf16 v[100:103], a[76:79], a[216:219], v[100:103]\n\tv_add_f32 v247, 1.0, v243\n\tv_fmac_f32 v244, v244, v20\n\tv_mfma_f32_16x16x32_bf16 v[104:107], a[80:83], a[216:219], v[104:107]\n\tv_fmac_f32 v245, v245, v21\n\tv_fmac_f32 v246, v246, v22\n\tv_mfma_f32_16x16x32_bf16 v[108:111], a[84:87], a[216:219], v[108:111]\n\tds_read_b128 a[216:219], v192 offset:34080\n\tv_fmac_f32 v247, v247, v23\n\tv_mfma_f32_16x16x32_bf16 v[96:99], a[96:99], a[220:223], v[96:99]\n\tv_rcp_f32 v20, v244\n\tv_mfma_f32_16x16x32_bf16 v[100:103], a[100:103], a[220:223], v[100:103]\n\tv_rcp_f32 v21, v245\n\tv_mfma_f32_16x16x32_bf16 v[104:107], a[104:107], a[220:223], v[104:107]\n\tv_rcp_f32 v22, v246\n\tv_mfma_f32_16x16x32_bf16 v[108:111], a[108:111], a[220:223], v[108:111]\n\tv_rcp_f32 v23, v247\n\tv_mfma_f32_16x16x32_bf16 v[96:99], a[120:123], a[224:227], v[96:99]\n\tv_fma_f32 v20, -v240, v20, v20\n\tv_fma_f32 v21, -v241, v21, v21\n\tv_mfma_f32_16x16x32_bf16 v[100:103], a[124:127], a[224:227], v[100:103]\n\tv_fma_f32 v22, -v242, v22, v22\n\tv_fma_f32 v23, -v243, v23, v23\n\tv_mfma_f32_16x16x32_bf16 v[104:107], a[128:131], a[224:227], v[104:107]\n\tv_bfi_b32 v20, s44, v20, v16\n\tv_bfi_b32 v21, s44, v21, v17\n\tv_mfma_f32_16x16x32_bf16 v[108:111], a[132:135], a[224:227], v[108:111]\n\tv_bfi_b32 v22, s44, v22, v18\n\tv_bfi_b32 v23, s44, v23, v19\n\tv_mfma_f32_16x16x32_bf16 v[96:99], a[144:147], a[228:231], v[96:99]\n\tv_mul_f32 v238, s42, v26\n\tv_mul_f32 v239, s42, v27\n\tv_mfma_f32_16x16x32_bf16 v[100:103], a[148:151], a[228:231], v[100:103]\n\tv_exp_f32_e64 v236, -|v236|\n\tv_mfma_f32_16x16x32_bf16 v[104:107], a[152:155], a[228:231], v[104:107]\n\tv_exp_f32_e64 v237, -|v237|\n\tv_mfma_f32_16x16x32_bf16 v[108:111], a[156:159], a[228:231], v[108:111]\n\tv_exp_f32_e64 v238, -|v238|\n\tv_mfma_f32_16x16x32_bf16 v[112:115], a[0:3], a[232:235], v[112:115]\n\tv_exp_f32_e64 v239, -|v239|\n\tv_mfma_f32_16x16x32_bf16 v[116:119], a[4:7], a[232:235], v[116:119]\n\tv_exp_f32 v28, v28\n\tv_mfma_f32_16x16x32_bf16 v[120:123], a[8:11], a[232:235], v[120:123]\n\tv_exp_f32 v29, v29\n\tv_mfma_f32_16x16x32_bf16 v[124:127], a[12:15], a[232:235], v[124:127]\n\tv_exp_f32 v30, v30\n\tv_mfma_f32_16x16x32_bf16 v[112:115], a[24:27], a[236:239], v[112:115]\n\tv_exp_f32 v31, v31\n\tv_mfma_f32_16x16x32_bf16 v[116:119], a[28:31], a[236:239], v[116:119]\n\tv_add_f32 v244, 1.0, v236\n\tv_add_f32 v245, 1.0, v237\n\tv_mfma_f32_16x16x32_bf16 v[120:123], a[32:35], a[236:239], v[120:123]\n\tv_add_f32 v246, 1.0, v238\n\tv_add_f32 v247, 1.0, v239\n\tv_mfma_f32_16x16x32_bf16 v[124:127], a[36:39], a[236:239], v[124:127]\n\ts_waitcnt lgkmcnt(0)\n\tv_fmac_f32 v244, v244, v28\n\tv_fmac_f32 v245, v245, v29\n\tv_mfma_f32_16x16x32_bf16 v[112:115], a[48:51], a[240:243], v[112:115]\n\tv_fmac_f32 v246, v246, v30\n\tv_fmac_f32 v247, v247, v31\n\tv_mfma_f32_16x16x32_bf16 v[116:119], a[52:55], a[240:243], v[116:119]\n\tv_rcp_f32 v28, v244\n\tv_mfma_f32_16x16x32_bf16 v[120:123], a[56:59], a[240:243], v[120:123]\n\tv_rcp_f32 v29, v245\n\tv_mfma_f32_16x16x32_bf16 v[124:127], a[60:63], a[240:243], v[124:127]\n\tv_rcp_f32 v30, v246\n\tv_mfma_f32_16x16x32_bf16 v[112:115], a[72:75], a[244:247], v[112:115]\n\tv_rcp_f32 v31, v247\n\tv_mfma_f32_16x16x32_bf16 v[116:119], a[76:79], a[244:247], v[116:119]\n\tv_fma_f32 v28, -v236, v28, v28\n\tv_fma_f32 v29, -v237, v29, v29\n\tv_mfma_f32_16x16x32_bf16 v[120:123], a[80:83], a[244:247], v[120:123]\n\tv_fma_f32 v30, -v238, v30, v30\n\tv_fma_f32 v31, -v239, v31, v31\n\tv_mfma_f32_16x16x32_bf16 v[124:127], a[84:87], a[244:247], v[124:127]\n\tv_bfi_b32 v28, s44, v28, v24\n\tv_bfi_b32 v29, s44, v29, v25\n\tv_mfma_f32_16x16x32_bf16 v[112:115], a[96:99], a[248:251], v[112:115]\n\tv_bfi_b32 v30, s44, v30, v26\n\tv_bfi_b32 v31, s44, v31, v27\n\tv_mfma_f32_16x16x32_bf16 v[116:119], a[100:103], a[248:251], v[116:119]\n\tv_cvt_pk_bf16_f32 v16, v20, v21\n\tv_cvt_pk_bf16_f32 v17, v22, v23\n\tv_mfma_f32_16x16x32_bf16 v[120:123], a[104:107], a[248:251], v[120:123]\n\tv_cvt_pk_bf16_f32 v18, v28, v29\n\tv_cvt_pk_bf16_f32 v19, v30, v31\n\tv_mfma_f32_16x16x32_bf16 v[124:127], a[108:111], a[248:251], v[124:127]\n\tds_write_b128 v214, v[16:19] offset:49664\n\tv_mul_f32 v240, s42, v32\n\tv_mfma_f32_16x16x32_bf16 v[112:115], a[120:123], a[252:255], v[112:115]\n\tv_mul_f32 v241, s42, v33\n\tv_mul_f32 v242, s42, v34\n\tv_mfma_f32_16x16x32_bf16 v[116:119], a[124:127], a[252:255], v[116:119]\n\tv_mul_f32 v243, s42, v35\n\tv_mul_f32 v236, s42, v40\n\tv_mfma_f32_16x16x32_bf16 v[120:123], a[128:131], a[252:255], v[120:123]\n\tv_exp_f32_e64 v240, -|v240|\n\tv_mfma_f32_16x16x32_bf16 v[124:127], a[132:135], a[252:255], v[124:127]\n\tv_exp_f32_e64 v241, -|v241|\n\tv_mfma_f32_16x16x32_bf16 v[112:115], a[144:147], a[216:219], v[112:115]\n\tv_exp_f32_e64 v242, -|v242|\n\tv_mfma_f32_16x16x32_bf16 v[116:119], a[148:151], a[216:219], v[116:119]\n\tv_exp_f32_e64 v243, -|v243|\n\tv_mfma_f32_16x16x32_bf16 v[120:123], a[152:155], a[216:219], v[120:123]\n\tv_exp_f32 v36, v36\n\tv_mfma_f32_16x16x32_bf16 v[124:127], a[156:159], a[216:219], v[124:127]\n\tv_exp_f32 v37, v37\n\tv_exp_f32 v38, v38\n\tv_exp_f32 v39, v39\n\tv_add_f32 v244, 1.0, v240\n\tv_add_f32 v245, 1.0, v241\n\tv_add_f32 v246, 1.0, v242\n\tv_add_f32 v247, 1.0, v243\n\tv_fmac_f32 v244, v244, v36\n\tv_fmac_f32 v245, v245, v37\n\tv_fmac_f32 v246, v246, v38\n\tv_fmac_f32 v247, v247, v39\n\tv_rcp_f32 v36, v244\n\tv_rcp_f32 v37, v245\n\tv_rcp_f32 v38, v246\n\tv_rcp_f32 v39, v247\n\tv_fma_f32 v36, -v240, v36, v36\n\tv_fma_f32 v37, -v241, v37, v37\n\tv_fma_f32 v38, -v242, v38, v38\n\tv_fma_f32 v39, -v243, v39, v39\n\tv_bfi_b32 v36, s44, v36, v32\n\tv_bfi_b32 v37, s44, v37, v33\n\tv_bfi_b32 v38, s44, v38, v34\n\tv_bfi_b32 v39, s44, v39, v35\n\tv_mul_f32 v237, s42, v41\n\tv_mul_f32 v238, s42, v42\n\tv_mul_f32 v239, s42, v43\n\tv_exp_f32_e64 v236, -|v236|\n\tv_exp_f32_e64 v237, -|v237|\n\tv_exp_f32_e64 v238, -|v238|\n\tv_exp_f32_e64 v239, -|v239|\n\tv_exp_f32 v44, v44\n\tv_exp_f32 v45, v45\n\tv_exp_f32 v46, v46\n\tv_exp_f32 v47, v47\n\tv_add_f32 v244, 1.0, v236\n\tv_add_f32 v245, 1.0, v237\n\tv_add_f32 v246, 1.0, v238\n\tv_add_f32 v247, 1.0, v239\n\tv_fmac_f32 v244, v244, v44\n\tv_fmac_f32 v245, v245, v45\n\tv_fmac_f32 v246, v246, v46\n\tv_fmac_f32 v247, v247, v47\n\tv_rcp_f32 v44, v244\n\tv_rcp_f32 v45, v245\n\tv_rcp_f32 v46, v246\n\tv_rcp_f32 v47, v247\n\tv_fma_f32 v44, -v236, v44, v44\n\tv_fma_f32 v45, -v237, v45, v45\n\tv_fma_f32 v46, -v238, v46, v46\n\tv_fma_f32 v47, -v239, v47, v47\n\tv_bfi_b32 v44, s44, v44, v40\n\tv_bfi_b32 v45, s44, v45, v41\n\tv_bfi_b32 v46, s44, v46, v42\n\tv_bfi_b32 v47, s44, v47, v43\n\tv_cvt_pk_bf16_f32 v32, v36, v37\n\tv_cvt_pk_bf16_f32 v33, v38, v39\n\tv_cvt_pk_bf16_f32 v34, v44, v45\n\tv_cvt_pk_bf16_f32 v35, v46, v47\n\tv_mul_f32 v240, s42, v48\n\tds_write_b128 v214, v[32:35] offset:54272\n\tv_mul_f32 v241, s42, v49\n\tv_mul_f32 v242, s42, v50\n\tv_mul_f32 v243, s42, v51\n\tv_exp_f32_e64 v240, -|v240|\n\tv_exp_f32_e64 v241, -|v241|\n\tv_exp_f32_e64 v242, -|v242|\n\tv_exp_f32_e64 v243, -|v243|\n\tv_exp_f32 v52, v52\n\tv_exp_f32 v53, v53\n\tv_exp_f32 v54, v54\n\tv_exp_f32 v55, v55\n\tv_add_f32 v244, 1.0, v240\n\tv_add_f32 v245, 1.0, v241\n\tv_add_f32 v246, 1.0, v242\n\tv_add_f32 v247, 1.0, v243\n\tv_fmac_f32 v244, v244, v52\n\tv_fmac_f32 v245, v245, v53\n\tv_fmac_f32 v246, v246, v54\n\tv_fmac_f32 v247, v247, v55\n\tv_rcp_f32 v52, v244\n\tv_rcp_f32 v53, v245\n\tv_rcp_f32 v54, v246\n\tv_rcp_f32 v55, v247\n\tv_fma_f32 v52, -v240, v52, v52\n\tv_fma_f32 v53, -v241, v53, v53\n\tv_fma_f32 v54, -v242, v54, v54\n\tv_fma_f32 v55, -v243, v55, v55\n\tv_bfi_b32 v52, s44, v52, v48\n\tv_bfi_b32 v53, s44, v53, v49\n\tv_bfi_b32 v54, s44, v54, v50\n\tv_bfi_b32 v55, s44, v55, v51\n\tv_mul_f32 v236, s42, v56\n\tv_mul_f32 v237, s42, v57\n\tv_mul_f32 v238, s42, v58\n\tv_mul_f32 v239, s42, v59\n\tv_exp_f32_e64 v236, -|v236|\n\tv_exp_f32_e64 v237, -|v237|\n\tv_exp_f32_e64 v238, -|v238|\n\tv_exp_f32_e64 v239, -|v239|\n\tv_exp_f32 v60, v60\n\tv_exp_f32 v61, v61\n\tv_exp_f32 v62, v62\n\tv_exp_f32 v63, v63\n\tv_add_f32 v244, 1.0, v236\n\tv_add_f32 v245, 1.0, v237\n\tv_add_f32 v246, 1.0, v238\n\tv_add_f32 v247, 1.0, v239\n\tv_fmac_f32 v244, v244, v60\n\tv_fmac_f32 v245, v245, v61\n\tv_fmac_f32 v246, v246, v62\n\tv_fmac_f32 v247, v247, v63\n\tv_rcp_f32 v60, v244\n\tv_rcp_f32 v61, v245\n\tv_rcp_f32 v62, v246\n\tv_rcp_f32 v63, v247\n\tv_fma_f32 v60, -v236, v60, v60\n\tv_fma_f32 v61, -v237, v61, v61\n\tv_fma_f32 v62, -v238, v62, v62\n\tv_fma_f32 v63, -v239, v63, v63\n\tv_bfi_b32 v60, s44, v60, v56\n\tv_bfi_b32 v61, s44, v61, v57\n\tv_bfi_b32 v62, s44, v62, v58\n\tv_bfi_b32 v63, s44, v63, v59\n\tv_cvt_pk_bf16_f32 v48, v52, v53\n\tv_cvt_pk_bf16_f32 v49, v54, v55\n\tv_cvt_pk_bf16_f32 v50, v60, v61\n\tv_cvt_pk_bf16_f32 v51, v62, v63\n\tv_mul_f32 v240, s42, v136\n\tds_write_b128 v214, v[48:51] offset:58880\n\tv_mul_f32 v241, s42, v137\n\tv_mul_f32 v242, s42, v138\n\tv_mul_f32 v243, s42, v139\n\tv_exp_f32_e64 v240, -|v240|\n\tv_exp_f32_e64 v241, -|v241|\n\tv_exp_f32_e64 v242, -|v242|\n\tv_exp_f32_e64 v243, -|v243|\n\tv_exp_f32 v140, v140\n\tv_exp_f32 v141, v141\n\tv_exp_f32 v142, v142\n\tv_exp_f32 v143, v143\n\tv_add_f32 v244, 1.0, v240\n\tv_add_f32 v245, 1.0, v241\n\tv_add_f32 v246, 1.0, v242\n\tv_add_f32 v247, 1.0, v243\n\tv_fmac_f32 v244, v244, v140\n\tv_fmac_f32 v245, v245, v141\n\tv_fmac_f32 v246, v246, v142\n\tv_fmac_f32 v247, v247, v143\n\ts_waitcnt lgkmcnt(0)\n\ts_barrier\n\tv_rcp_f32 v140, v244\n\tds_read_b128 v[144:147], v213 offset:45056\n\tv_rcp_f32 v141, v245\n\tv_rcp_f32 v142, v246\n\tds_read_b128 v[148:151], v213 offset:46080\n\tv_rcp_f32 v143, v247\n\tv_fma_f32 v140, -v240, v140, v140\n\tds_read_b128 v[152:155], v213 offset:47104\n\tv_fma_f32 v141, -v241, v141, v141\n\tv_fma_f32 v142, -v242, v142, v142\n\tv_fma_f32 v143, -v243, v143, v143\n\tds_read_b128 v[156:159], v213 offset:48128\n\tv_bfi_b32 v140, s44, v140, v136\n\tv_bfi_b32 v141, s44, v141, v137\n\tv_bfi_b32 v142, s44, v142, v138\n\tds_read_b64 v[160:161], v215 offset:49152\n\tv_bfi_b32 v143, s44, v143, v139\n\tv_cvt_pk_bf16_f32 v136, v140, v141\n\tds_read_b128 v[168:171], v213 offset:49664\n\tv_cvt_pk_bf16_f32 v137, v142, v143\n\tv_mul_f32 v236, s42, v64\n\ts_waitcnt lgkmcnt(5)\n\tv_mfma_f32_16x16x32_bf16 v[4:7], a[16:19], v[144:147], 0\n\tds_read_b128 v[172:175], v213 offset:50688\n\tv_mul_f32 v237, s42, v65\n\tv_mfma_f32_16x16x32_bf16 v[12:15], a[64:67], v[144:147], 0\n\ts_waitcnt lgkmcnt(5)\n\tds_read_b128 v[176:179], v213 offset:51712\n\tv_mul_f32 v238, s42, v66\n\tv_mfma_f32_16x16x32_bf16 v[4:7], a[20:23], v[148:151], v[4:7]\n\tds_read_b128 v[180:183], v213 offset:52736\n\tv_mul_f32 v239, s42, v67\n\tv_mfma_f32_16x16x32_bf16 v[12:15], a[68:71], v[148:151], v[12:15]\n\ts_waitcnt lgkmcnt(5)\n\tds_read_b64 v[184:185], v215 offset:53760\n\tv_mul_f32 v68, s43, v68\n\tv_mfma_f32_16x16x32_bf16 v[4:7], a[40:43], v[152:155], v[4:7]\n\tds_read_b128 v[144:147], v213 offset:54272\n\tv_mul_f32 v69, s43, v69\n\tv_mfma_f32_16x16x32_bf16 v[12:15], a[88:91], v[152:155], v[12:15]\n\tds_read_b128 v[148:151], v213 offset:55296\n\tv_mul_f32 v70, s43, v70\n\tv_mfma_f32_16x16x32_bf16 v[4:7], a[44:47], v[156:159], v[4:7]\n\tds_read_b128 v[152:155], v213 offset:56320\n\tv_mul_f32 v71, s43, v71\n\tv_mfma_f32_16x16x32_bf16 v[12:15], a[92:95], v[156:159], v[12:15]\n\ts_waitcnt lgkmcnt(5)\n\tds_read_b128 v[156:159], v213 offset:57344\n\tv_mul_f32 v240, s42, v72\n\tv_mfma_f32_16x16x32_bf16 v[20:23], a[16:19], v[168:171], 0\n\tds_write_b64 v224, v[136:137] offset:21504\n\tv_mul_f32 v241, s42, v73\n\tv_mfma_f32_16x16x16_bf16 v[4:7], v[250:251], v[160:161], v[4:7]\n\tv_exp_f32_e64 v236, -|v236|\n\tv_mfma_f32_16x16x16_bf16 v[12:15], v[252:253], v[160:161], v[12:15]\n\tds_read_b64 v[160:161], v215 offset:58368\n\tv_add_f32 v244, 1.0, v236\n\tv_mfma_f32_16x16x32_bf16 v[28:31], a[64:67], v[168:171], 0\n\tds_read_b128 v[168:171], v213 offset:58880\n\tv_mul_f32 v242, s42, v74\n\tv_mfma_f32_16x16x32_bf16 v[20:23], a[20:23], v[172:175], v[20:23]\n\tv_cvt_pk_bf16_f32 v8, v4, v5\n\tv_cvt_pk_bf16_f32 v9, v6, v7\n\tv_mfma_f32_16x16x32_bf16 v[28:31], a[68:71], v[172:175], v[28:31]\n\tds_read_b128 v[172:175], v213 offset:59904\n\tv_cvt_pk_bf16_f32 v10, v12, v13\n\tv_mfma_f32_16x16x32_bf16 v[20:23], a[40:43], v[176:179], v[20:23]\n\tv_cvt_pk_bf16_f32 v11, v14, v15\n\tv_mul_f32 v243, s42, v75\n\tv_mfma_f32_16x16x32_bf16 v[28:31], a[88:91], v[176:179], v[28:31]\n\ts_waitcnt lgkmcnt(3)\n\tds_write_b128 v225, v[8:11] offset:45056\n\tv_mul_f32 v76, s43, v76\n\tv_mfma_f32_16x16x32_bf16 v[20:23], a[44:47], v[180:183], v[20:23]\n\tv_exp_f32_e64 v237, -|v237|\n\tv_mfma_f32_16x16x32_bf16 v[28:31], a[92:95], v[180:183], v[28:31]\n\tds_read_b128 v[176:179], v213 offset:60928\n\tv_add_f32 v245, 1.0, v237\n\tv_mfma_f32_16x16x32_bf16 v[36:39], a[16:19], v[144:147], 0\n\tds_read_b128 v[180:183], v213 offset:61952\n\tv_mul_f32 v77, s43, v77\n\tv_mfma_f32_16x16x16_bf16 v[20:23], v[250:251], v[184:185], v[20:23]\n\tv_exp_f32_e64 v238, -|v238|\n\tv_mfma_f32_16x16x16_bf16 v[28:31], v[252:253], v[184:185], v[28:31]\n\tds_read_b64 v[184:185], v215 offset:62976\n\tv_add_f32 v246, 1.0, v238\n\tv_mfma_f32_16x16x32_bf16 v[44:47], a[64:67], v[144:147], 0\n\tds_read_b128 v[144:147], v211 offset:45056\n\tv_mul_f32 v78, s43, v78\n\tv_mfma_f32_16x16x32_bf16 v[36:39], a[20:23], v[148:151], v[36:39]\n\tv_cvt_pk_bf16_f32 v24, v20, v21\n\tv_cvt_pk_bf16_f32 v25, v22, v23\n\tv_mfma_f32_16x16x32_bf16 v[44:47], a[68:71], v[148:151], v[44:47]\n\tds_read_b128 v[148:151], v211 offset:46080\n\tv_cvt_pk_bf16_f32 v26, v28, v29\n\tv_mfma_f32_16x16x32_bf16 v[36:39], a[40:43], v[152:155], v[36:39]\n\tv_cvt_pk_bf16_f32 v27, v30, v31\n\tv_mul_f32 v79, s43, v79\n\tv_mfma_f32_16x16x32_bf16 v[44:47], a[88:91], v[152:155], v[44:47]\n\tds_write_b128 v225, v[24:27] offset:49664\n\tv_mul_f32 v84, s43, v84\n\tv_mfma_f32_16x16x32_bf16 v[36:39], a[44:47], v[156:159], v[36:39]\n\tv_exp_f32_e64 v239, -|v239|\n\tv_mfma_f32_16x16x32_bf16 v[44:47], a[92:95], v[156:159], v[44:47]\n\ts_waitcnt lgkmcnt(2)\n\tds_read_b128 v[152:155], v211 offset:47104\n\tv_add_f32 v247, 1.0, v239\n\tv_mfma_f32_16x16x32_bf16 v[52:55], a[16:19], v[168:171], 0\n\tds_read_b128 v[156:159], v211 offset:48128\n\tv_mul_f32 v85, s43, v85\n\tv_mfma_f32_16x16x16_bf16 v[36:39], v[250:251], v[160:161], v[36:39]\n\tv_exp_f32 v68, v68\n\tv_mfma_f32_16x16x16_bf16 v[44:47], v[252:253], v[160:161], v[44:47]\n\tds_read_b64 v[160:161], v216 offset:49152\n\tv_fmac_f32 v244, v244, v68\n\tv_mfma_f32_16x16x32_bf16 v[60:63], a[64:67], v[168:171], 0\n\tv_exp_f32 v69, v69\n\tv_mfma_f32_16x16x32_bf16 v[52:55], a[20:23], v[172:175], v[52:55]\n\ts_nop 0\n\tv_cvt_pk_bf16_f32 v40, v36, v37\n\tv_cvt_pk_bf16_f32 v41, v38, v39\n\tv_mfma_f32_16x16x32_bf16 v[60:63], a[68:71], v[172:175], v[60:63]\n\tv_cvt_pk_bf16_f32 v42, v44, v45\n\tv_cvt_pk_bf16_f32 v43, v46, v47\n\tv_mfma_f32_16x16x32_bf16 v[52:55], a[40:43], v[176:179], v[52:55]\n\tds_write_b128 v225, v[40:43] offset:54272\n\tv_fmac_f32 v245, v245, v69\n\tv_mfma_f32_16x16x32_bf16 v[60:63], a[88:91], v[176:179], v[60:63]\n\tv_exp_f32 v70, v70\n\tv_mfma_f32_16x16x32_bf16 v[52:55], a[44:47], v[180:183], v[52:55]\n\tv_exp_f32 v71, v71\n\tv_mfma_f32_16x16x32_bf16 v[60:63], a[92:95], v[180:183], v[60:63]\n\tv_fmac_f32 v246, v246, v70\n\tv_fmac_f32 v247, v247, v71\n\tv_mfma_f32_16x16x32_bf16 v[132:135], a[112:115], v[144:147], 0\n\tv_rcp_f32 v68, v244\n\tv_mfma_f32_16x16x16_bf16 v[52:55], v[250:251], v[184:185], v[52:55]\n\tv_rcp_f32 v69, v245\n\tv_mfma_f32_16x16x16_bf16 v[60:63], v[252:253], v[184:185], v[60:63]\n\ts_waitcnt lgkmcnt(0)\n\tv_rcp_f32 v70, v246\n\tv_mfma_f32_16x16x32_bf16 v[132:135], a[116:119], v[148:151], v[132:135]\n\tv_rcp_f32 v71, v247\n\tv_mfma_f32_16x16x32_bf16 v[132:135], a[136:139], v[152:155], v[132:135]\n\ts_nop 0\n\tv_cvt_pk_bf16_f32 v56, v52, v53\n\tv_cvt_pk_bf16_f32 v57, v54, v55\n\tv_mfma_f32_16x16x32_bf16 v[132:135], a[140:143], v[156:159], v[132:135]\n\tv_cvt_pk_bf16_f32 v58, v60, v61\n\tv_cvt_pk_bf16_f32 v59, v62, v63\n\tv_fma_f32 v68, -v236, v68, v68\n\tds_write_b128 v225, v[56:59] offset:58880\n\tv_fma_f32 v69, -v237, v69, v69\n\tv_fma_f32 v70, -v238, v70, v70\n\tv_fma_f32 v71, -v239, v71, v71\n\tv_bfi_b32 v68, s44, v68, v64\n\tv_mfma_f32_16x16x16_bf16 v[132:135], v[254:255], v[160:161], v[132:135]\n\tv_bfi_b32 v69, s44, v69, v65\n\tv_bfi_b32 v70, s44, v70, v66\n\tv_bfi_b32 v71, s44, v71, v67\n\tv_exp_f32_e64 v240, -|v240|\n\tv_exp_f32_e64 v241, -|v241|\n\tv_exp_f32_e64 v242, -|v242|\n\tv_exp_f32_e64 v243, -|v243|\n\tv_exp_f32 v76, v76\n\tv_cvt_pk_bf16_f32 v130, v132, v133\n\tv_cvt_pk_bf16_f32 v131, v134, v135\n\tv_exp_f32 v77, v77\n\tds_write_b64 v212, v[130:131] offset:45312\n\tv_exp_f32 v78, v78\n\tv_exp_f32 v79, v79\n\tv_add_f32 v244, 1.0, v240\n\tv_add_f32 v245, 1.0, v241\n\tv_add_f32 v246, 1.0, v242\n\tv_add_f32 v247, 1.0, v243\n\tv_fmac_f32 v244, v244, v76\n\tv_fmac_f32 v245, v245, v77\n\tv_fmac_f32 v246, v246, v78\n\tv_fmac_f32 v247, v247, v79\n\tv_rcp_f32 v76, v244\n\tv_rcp_f32 v77, v245\n\tv_rcp_f32 v78, v246\n\tv_rcp_f32 v79, v247\n\tv_fma_f32 v76, -v240, v76, v76\n\tv_fma_f32 v77, -v241, v77, v77\n\tv_fma_f32 v78, -v242, v78, v78\n\tv_fma_f32 v79, -v243, v79, v79\n\tv_bfi_b32 v76, s44, v76, v72\n\tv_bfi_b32 v77, s44, v77, v73\n\tv_bfi_b32 v78, s44, v78, v74\n\tv_bfi_b32 v79, s44, v79, v75\n\tv_cvt_pk_bf16_f32 v64, v68, v69\n\tv_cvt_pk_bf16_f32 v65, v70, v71\n\tv_cvt_pk_bf16_f32 v66, v76, v77\n\tv_cvt_pk_bf16_f32 v67, v78, v79\n\tv_mul_f32 v236, s42, v80\n\tds_write_b128 v222, v[64:67] offset:17408\n\tv_mul_f32 v237, s42, v81\n\tv_mul_f32 v238, s42, v82\n\tv_mul_f32 v239, s42, v83\n\tv_mul_f32 v86, s43, v86\n\tv_mul_f32 v87, s43, v87\n\tv_exp_f32_e64 v236, -|v236|\n\tv_exp_f32_e64 v237, -|v237|\n\tv_exp_f32_e64 v238, -|v238|\n\tv_exp_f32_e64 v239, -|v239|\n\tv_exp_f32 v84, v84\n\tv_exp_f32 v85, v85\n\tv_exp_f32 v86, v86\n\tv_exp_f32 v87, v87\n\tv_add_f32 v244, 1.0, v236\n\tv_add_f32 v245, 1.0, v237\n\tv_add_f32 v246, 1.0, v238\n\tv_add_f32 v247, 1.0, v239\n\tv_fmac_f32 v244, v244, v84\n\tv_fmac_f32 v245, v245, v85\n\tv_fmac_f32 v246, v246, v86\n\tv_fmac_f32 v247, v247, v87\n\tv_rcp_f32 v84, v244\n\tv_rcp_f32 v85, v245\n\tv_rcp_f32 v86, v246\n\tv_rcp_f32 v87, v247\n\tv_fma_f32 v84, -v236, v84, v84\n\tv_fma_f32 v85, -v237, v85, v85\n\tv_fma_f32 v86, -v238, v86, v86\n\tv_fma_f32 v87, -v239, v87, v87\n\tv_bfi_b32 v84, s44, v84, v80\n\tv_bfi_b32 v85, s44, v85, v81\n\tv_bfi_b32 v86, s44, v86, v82\n\tv_bfi_b32 v87, s44, v87, v83\n\tv_mul_f32 v240, s42, v88\n\tv_mul_f32 v241, s42, v89\n\tv_mul_f32 v242, s42, v90\n\tv_mul_f32 v243, s42, v91\n\tv_mul_f32 v92, s43, v92\n\tv_mul_f32 v93, s43, v93\n\tv_mul_f32 v94, s43, v94\n\tv_mul_f32 v95, s43, v95\n\tv_exp_f32_e64 v240, -|v240|\n\tv_exp_f32_e64 v241, -|v241|\n\tv_exp_f32_e64 v242, -|v242|\n\tv_exp_f32_e64 v243, -|v243|\n\tv_exp_f32 v92, v92\n\tv_exp_f32 v93, v93\n\tv_exp_f32 v94, v94\n\tv_exp_f32 v95, v95\n\tv_add_f32 v244, 1.0, v240\n\tv_add_f32 v245, 1.0, v241\n\tv_add_f32 v246, 1.0, v242\n\tv_add_f32 v247, 1.0, v243\n\tv_fmac_f32 v244, v244, v92\n\tv_fmac_f32 v245, v245, v93\n\tv_fmac_f32 v246, v246, v94\n\tv_fmac_f32 v247, v247, v95\n\tv_rcp_f32 v92, v244\n\tv_rcp_f32 v93, v245\n\tv_rcp_f32 v94, v246\n\tv_rcp_f32 v95, v247\n\tv_fma_f32 v92, -v240, v92, v92\n\tv_fma_f32 v93, -v241, v93, v93\n\tv_fma_f32 v94, -v242, v94, v94\n\tv_fma_f32 v95, -v243, v95, v95\n\tv_bfi_b32 v92, s44, v92, v88\n\tv_bfi_b32 v93, s44, v93, v89\n\tv_bfi_b32 v94, s44, v94, v90\n\tv_bfi_b32 v95, s44, v95, v91\n\tv_cvt_pk_bf16_f32 v80, v84, v85\n\tv_cvt_pk_bf16_f32 v81, v86, v87\n\tv_cvt_pk_bf16_f32 v82, v92, v93\n\tv_cvt_pk_bf16_f32 v83, v94, v95\n\tv_mul_f32 v236, s42, v96\n\tds_write_b128 v222, v[80:83] offset:22016\n\tv_mul_f32 v237, s42, v97\n\tv_mul_f32 v238, s42, v98\n\tv_mul_f32 v239, s42, v99\n\tv_mul_f32 v100, s43, v100\n\tv_mul_f32 v101, s43, v101\n\tv_mul_f32 v102, s43, v102\n\tv_mul_f32 v103, s43, v103\n\tv_exp_f32_e64 v236, -|v236|\n\tv_exp_f32_e64 v237, -|v237|\n\tv_exp_f32_e64 v238, -|v238|\n\tv_exp_f32_e64 v239, -|v239|\n\tv_exp_f32 v100, v100\n\tv_exp_f32 v101, v101\n\tv_exp_f32 v102, v102\n\tv_exp_f32 v103, v103\n\tv_add_f32 v244, 1.0, v236\n\tv_add_f32 v245, 1.0, v237\n\tv_add_f32 v246, 1.0, v238\n\tv_add_f32 v247, 1.0, v239\n\ts_waitcnt lgkmcnt(0)\n\ts_barrier\n\tv_fmac_f32 v244, v244, v100\n\tv_fmac_f32 v245, v245, v101\n\tds_read_b128 v[168:171], v221 offset:17408\n\tv_fmac_f32 v246, v246, v102\n\tv_fmac_f32 v247, v247, v103\n\tv_rcp_f32 v100, v244\n\tds_read_b128 v[172:175], v221 offset:18432\n\tv_rcp_f32 v101, v245\n\tv_rcp_f32 v102, v246\n\tds_read_b128 v[176:179], v221 offset:19456\n\tv_rcp_f32 v103, v247\n\tv_fma_f32 v100, -v236, v100, v100\n\tds_read_b128 v[180:183], v221 offset:20480\n\tv_fma_f32 v101, -v237, v101, v101\n\tv_fma_f32 v102, -v238, v102, v102\n\tv_fma_f32 v103, -v239, v103, v103\n\tds_read_b64 v[184:185], v223 offset:21504\n\tv_bfi_b32 v100, s44, v100, v96\n\tv_bfi_b32 v101, s44, v101, v97\n\tds_read_b128 v[144:147], v221 offset:22016\n\tv_bfi_b32 v102, s44, v102, v98\n\ts_waitcnt lgkmcnt(5)\n\tv_mfma_f32_16x16x32_bf16 v[68:71], a[16:19], v[168:171], 0\n\tds_read_b128 v[148:151], v221 offset:23040\n\tv_bfi_b32 v103, s44, v103, v99\n\tv_mfma_f32_16x16x32_bf16 v[76:79], a[64:67], v[168:171], 0\n\tds_read_b128 v[152:155], v221 offset:24064\n\tv_mul_f32 v240, s42, v104\n\tv_mfma_f32_16x16x32_bf16 v[64:67], a[112:115], v[168:171], 0\n\ts_waitcnt lgkmcnt(5)\n\tds_read_b128 v[156:159], v221 offset:25088\n\tv_mul_f32 v241, s42, v105\n\tv_mfma_f32_16x16x32_bf16 v[68:71], a[20:23], v[172:175], v[68:71]\n\tds_read_b64 v[160:161], v223 offset:26112\n\tv_mul_f32 v242, s42, v106\n\tv_mfma_f32_16x16x32_bf16 v[76:79], a[68:71], v[172:175], v[76:79]\n\tds_read_b128 a[168:171], v232 offset:45056\n\tv_mul_f32 v243, s42, v107\n\tv_mfma_f32_16x16x32_bf16 v[64:67], a[116:119], v[172:175], v[64:67]\n\tds_read_b128 a[172:175], v232 offset:46080\n\tv_mul_f32 v108, s43, v108\n\tv_mfma_f32_16x16x32_bf16 v[68:71], a[40:43], v[176:179], v[68:71]\n\tds_read_b128 a[176:179], v232 offset:47104\n\tv_mul_f32 v109, s43, v109\n\tv_mfma_f32_16x16x32_bf16 v[76:79], a[88:91], v[176:179], v[76:79]\n\tds_read_b128 a[180:183], v232 offset:54272\n\tv_mul_f32 v110, s43, v110\n\tv_mfma_f32_16x16x32_bf16 v[64:67], a[136:139], v[176:179], v[64:67]\n\ts_waitcnt lgkmcnt(5)\n\tds_read_b128 a[184:187], v232 offset:55296\n\tv_mul_f32 v111, s43, v111\n\tv_mfma_f32_16x16x32_bf16 v[68:71], a[44:47], v[180:183], v[68:71]\n\tv_exp_f32_e64 v240, -|v240|\n\tv_mfma_f32_16x16x32_bf16 v[76:79], a[92:95], v[180:183], v[76:79]\n\ts_waitcnt lgkmcnt(4)\n\tbuffer_store_dwordx4 a[168:171], v233, s[28:31], s41 offen sc1\n\tv_add_f32 v244, 1.0, v240\n\tv_mfma_f32_16x16x32_bf16 v[64:67], a[140:143], v[180:183], v[64:67]\n\ts_mov_b32 s41, 1024\n\tds_read_b128 a[168:171], v232 offset:56320\n\tv_mfma_f32_16x16x16_bf16 v[68:71], v[250:251], v[184:185], v[68:71]\n\tv_exp_f32_e64 v241, -|v241|\n\tv_mfma_f32_16x16x16_bf16 v[76:79], v[252:253], v[184:185], v[76:79]\n\tv_exp_f32_e64 v242, -|v242|\n\tv_mfma_f32_16x16x16_bf16 v[64:67], v[254:255], v[184:185], v[64:67]\n\tv_exp_f32_e64 v243, -|v243|\n\tv_mfma_f32_16x16x32_bf16 v[84:87], a[16:19], v[144:147], 0\n\ts_nop 1\n\tv_cvt_pk_bf16_f32 v72, v68, v69\n\tv_cvt_pk_bf16_f32 v73, v70, v71\n\tv_mfma_f32_16x16x32_bf16 v[92:95], a[64:67], v[144:147], 0\n\ts_waitcnt lgkmcnt(1)\n\tbuffer_store_dwordx4 a[172:175], v233, s[28:31], s41 offen sc1\n\tv_cvt_pk_bf16_f32 v74, v76, v77\n\tv_mfma_f32_16x16x32_bf16 v[80:83], a[112:115], v[144:147], 0\n\ts_mov_b32 s41, 2048\n\tv_cvt_pk_bf16_f32 v75, v78, v79\n\tv_mfma_f32_16x16x32_bf16 v[84:87], a[20:23], v[148:151], v[84:87]\n\tds_write_b128 v219, v[72:75] offset:64512\n\tv_cvt_pk_bf16_f32 v64, v64, v65\n\tv_mfma_f32_16x16x32_bf16 v[92:95], a[68:71], v[148:151], v[92:95]\n\tv_cvt_pk_bf16_f32 v65, v66, v67\n\tv_add_f32 v245, 1.0, v241\n\tv_mfma_f32_16x16x32_bf16 v[80:83], a[116:119], v[148:151], v[80:83]\n\tds_write_b64 v220, v[64:65] offset:64768\n\tv_add_f32 v246, 1.0, v242\n\tv_mfma_f32_16x16x32_bf16 v[84:87], a[40:43], v[152:155], v[84:87]\n\tv_exp_f32 v108, v108\n\tv_mfma_f32_16x16x32_bf16 v[92:95], a[88:91], v[152:155], v[92:95]\n\tbuffer_store_dwordx4 a[176:179], v234, s[28:31], s41 offen sc1\n\tv_add_f32 v247, 1.0, v243\n\tv_mfma_f32_16x16x32_bf16 v[80:83], a[136:139], v[152:155], v[80:83]\n\ts_mov_b32 s41, 9216\n\tv_fmac_f32 v244, v244, v108\n\tv_mfma_f32_16x16x32_bf16 v[84:87], a[44:47], v[156:159], v[84:87]\n\tv_exp_f32 v109, v109\n\tv_mfma_f32_16x16x32_bf16 v[92:95], a[92:95], v[156:159], v[92:95]\n\tv_exp_f32 v110, v110\n\tv_mfma_f32_16x16x32_bf16 v[80:83], a[140:143], v[156:159], v[80:83]\n\tv_exp_f32 v111, v111\n\ts_nop 0\n\tv_mfma_f32_16x16x16_bf16 v[84:87], v[250:251], v[160:161], v[84:87]\n\tv_fmac_f32 v245, v245, v109\n\tv_fmac_f32 v246, v246, v110\n\tv_mfma_f32_16x16x16_bf16 v[92:95], v[252:253], v[160:161], v[92:95]\n\tbuffer_store_dwordx4 a[180:183], v233, s[28:31], s41 offen sc1\n\tv_fmac_f32 v247, v247, v111\n\tv_mfma_f32_16x16x16_bf16 v[80:83], v[254:255], v[160:161], v[80:83]\n\ts_mov_b32 s41, 10240\n\tv_rcp_f32 v108, v244\n\tv_rcp_f32 v109, v245\n\tv_cvt_pk_bf16_f32 v88, v84, v85\n\tv_cvt_pk_bf16_f32 v89, v86, v87\n\tv_rcp_f32 v110, v246\n\tv_cvt_pk_bf16_f32 v90, v92, v93\n\tv_cvt_pk_bf16_f32 v91, v94, v95\n\tv_rcp_f32 v111, v247\n\tds_write_b128 v225, v[88:91] offset:3584\n\tv_cvt_pk_bf16_f32 v80, v80, v81\n\tv_cvt_pk_bf16_f32 v81, v82, v83\n\tv_fma_f32 v108, -v240, v108, v108\n\tv_fma_f32 v109, -v241, v109, v109\n\tv_fma_f32 v110, -v242, v110, v110\n\tbuffer_store_dwordx4 a[184:187], v233, s[28:31], s41 offen sc1\n\tv_fma_f32 v111, -v243, v111, v111\n\tds_write_b64 v226, v[80:81] offset:3840\n\tv_bfi_b32 v108, s44, v108, v104\n\tv_bfi_b32 v109, s44, v109, v105\n\ts_mov_b32 s41, 11264\n\tv_bfi_b32 v110, s44, v110, v106\n\tv_bfi_b32 v111, s44, v111, v107\n\tv_cvt_pk_bf16_f32 v96, v100, v101\n\tv_cvt_pk_bf16_f32 v97, v102, v103\n\tv_cvt_pk_bf16_f32 v98, v108, v109\n\tv_cvt_pk_bf16_f32 v99, v110, v111\n\tv_mul_f32 v236, s42, v112\n\tds_write_b128 v222, v[96:99] offset:26624\n\tv_mul_f32 v237, s42, v113\n\tv_mul_f32 v238, s42, v114\n\tv_mul_f32 v239, s42, v115\n\tv_mul_f32 v116, s43, v116\n\tv_mul_f32 v117, s43, v117\n\tv_mul_f32 v118, s43, v118\n\tv_mul_f32 v119, s43, v119\n\tv_exp_f32_e64 v236, -|v236|\n\ts_waitcnt lgkmcnt(2)\n\tbuffer_store_dwordx4 a[168:171], v234, s[28:31], s41 offen sc1\n\tv_exp_f32_e64 v237, -|v237|\n\tv_exp_f32_e64 v238, -|v238|\n\ts_mov_b32 s41, 18432\n\tv_exp_f32_e64 v239, -|v239|\n\tv_exp_f32 v116, v116\n\tv_exp_f32 v117, v117\n\tv_exp_f32 v118, v118\n\tv_exp_f32 v119, v119\n\tv_add_f32 v244, 1.0, v236\n\tv_add_f32 v245, 1.0, v237\n\ts_waitcnt lgkmcnt(0)\n\ts_barrier\n\tv_add_f32 v246, 1.0, v238\n\tv_add_f32 v247, 1.0, v239\n\tds_read_b128 v[168:171], v221 offset:26624\n\tv_fmac_f32 v244, v244, v116\n\tv_fmac_f32 v245, v245, v117\n\tv_fmac_f32 v246, v246, v118\n\tds_read_b128 v[172:175], v221 offset:27648\n\tv_fmac_f32 v247, v247, v119\n\tv_rcp_f32 v116, v244\n\tds_read_b128 v[176:179], v221 offset:28672\n\tv_rcp_f32 v117, v245\n\tv_rcp_f32 v118, v246\n\tds_read_b128 v[180:183], v221 offset:29696\n\tv_rcp_f32 v119, v247\n\tv_fma_f32 v116, -v236, v116, v116\n\tds_read_b64 v[184:185], v223 offset:30720\n\tv_fma_f32 v117, -v237, v117, v117\n\tv_fma_f32 v118, -v238, v118, v118\n\tds_read_b128 a[172:175], v231 offset:64512\n\tv_fma_f32 v119, -v239, v119, v119\n\tv_bfi_b32 v116, s44, v116, v112\n\ts_waitcnt lgkmcnt(5)\n\tv_mfma_f32_16x16x32_bf16 v[100:103], a[16:19], v[168:171], 0\n\tds_read_b128 a[176:179], v232 offset:0\n\tv_bfi_b32 v117, s44, v117, v113\n\tv_mfma_f32_16x16x32_bf16 v[108:111], a[64:67], v[168:171], 0\n\tds_read_b128 a[180:183], v232 offset:1024\n\tv_bfi_b32 v118, s44, v118, v114\n\tv_mfma_f32_16x16x32_bf16 v[96:99], a[112:115], v[168:171], 0\n\ts_waitcnt lgkmcnt(5)\n\tv_bfi_b32 v119, s44, v119, v115\n\tv_mul_f32 v240, s42, v120\n\tv_mfma_f32_16x16x32_bf16 v[100:103], a[20:23], v[172:175], v[100:103]\n\tv_mul_f32 v241, s42, v121\n\tv_mul_f32 v242, s42, v122\n\tv_mfma_f32_16x16x32_bf16 v[108:111], a[68:71], v[172:175], v[108:111]\n\tv_mul_f32 v243, s42, v123\n\tv_mul_f32 v124, s43, v124\n\tv_mfma_f32_16x16x32_bf16 v[96:99], a[116:119], v[172:175], v[96:99]\n\ts_waitcnt lgkmcnt(2)\n\tbuffer_store_dwordx4 a[172:175], v233, s[28:31], s41 offen sc1\n\tv_mul_f32 v125, s43, v125\n\tv_mfma_f32_16x16x32_bf16 v[100:103], a[40:43], v[176:179], v[100:103]\n\ts_mov_b32 s41, 19456\n\tv_mul_f32 v126, s43, v126\n\tv_mfma_f32_16x16x32_bf16 v[108:111], a[88:91], v[176:179], v[108:111]\n\tv_mul_f32 v127, s43, v127\n\tv_cvt_pk_bf16_f32 v112, v116, v117\n\tv_mfma_f32_16x16x32_bf16 v[96:99], a[136:139], v[176:179], v[96:99]\n\tv_exp_f32_e64 v240, -|v240|\n\tv_mfma_f32_16x16x32_bf16 v[100:103], a[44:47], v[180:183], v[100:103]\n\tv_exp_f32_e64 v241, -|v241|\n\tv_mfma_f32_16x16x32_bf16 v[108:111], a[92:95], v[180:183], v[108:111]\n\tv_exp_f32_e64 v242, -|v242|\n\tv_mfma_f32_16x16x32_bf16 v[96:99], a[140:143], v[180:183], v[96:99]\n\ts_waitcnt lgkmcnt(0)\n\tbuffer_store_dwordx4 a[176:179], v233, s[28:31], s41 offen sc1\n\tv_add_f32 v244, 1.0, v240\n\tv_mfma_f32_16x16x16_bf16 v[100:103], v[250:251], v[184:185], v[100:103]\n\ts_mov_b32 s41, 20480\n\tv_add_f32 v245, 1.0, v241\n\tv_mfma_f32_16x16x16_bf16 v[108:111], v[252:253], v[184:185], v[108:111]\n\tv_exp_f32_e64 v243, -|v243|\n\tv_mfma_f32_16x16x16_bf16 v[96:99], v[254:255], v[184:185], v[96:99]\n\tv_exp_f32 v124, v124\n\tv_exp_f32 v125, v125\n\ts_nop 0\n\tv_cvt_pk_bf16_f32 v104, v100, v101\n\tv_cvt_pk_bf16_f32 v105, v102, v103\n\tv_exp_f32 v126, v126\n\tv_cvt_pk_bf16_f32 v106, v108, v109\n\tv_cvt_pk_bf16_f32 v107, v110, v111\n\tv_exp_f32 v127, v127\n\tds_write_b128 v225, v[104:107] offset:8192\n\tbuffer_store_dwordx4 a[180:183], v234, s[28:31], s41 offen sc1\n\tv_cvt_pk_bf16_f32 v96, v96, v97\n\tv_cvt_pk_bf16_f32 v97, v98, v99\n\tv_add_f32 v246, 1.0, v242\n\ts_mov_b32 s41, 27648\n\tv_add_f32 v247, 1.0, v243\n\tv_fmac_f32 v244, v244, v124\n\tds_write_b64 v226, v[96:97] offset:8448\n\tv_fmac_f32 v245, v245, v125\n\tv_fmac_f32 v246, v246, v126\n\tv_fmac_f32 v247, v247, v127\n\tv_rcp_f32 v124, v244\n\tv_rcp_f32 v125, v245\n\tv_rcp_f32 v126, v246\n\tv_rcp_f32 v127, v247\n\tv_fma_f32 v124, -v240, v124, v124\n\tv_fma_f32 v125, -v241, v125, v125\n\tv_fma_f32 v126, -v242, v126, v126\n\tv_fma_f32 v127, -v243, v127, v127\n\tv_bfi_b32 v124, s44, v124, v120\n\tv_bfi_b32 v125, s44, v125, v121\n\tv_bfi_b32 v126, s44, v126, v122\n\tv_bfi_b32 v127, s44, v127, v123\n\tv_cvt_pk_bf16_f32 v113, v118, v119\n\tv_cvt_pk_bf16_f32 v114, v124, v125\n\tv_cvt_pk_bf16_f32 v115, v126, v127\n\tds_write_b128 v222, v[112:115] offset:31232\n\ts_waitcnt lgkmcnt(0)\n\ts_barrier\n\tds_read_b128 v[144:147], v221 offset:31232\n\tds_read_b128 v[148:151], v221 offset:32256\n\tds_read_b128 v[152:155], v221 offset:33280\n\tds_read_b128 v[156:159], v221 offset:34304\n\tds_read_b64 v[160:161], v223 offset:35328\n\tds_read_b128 a[184:187], v228 offset:8192\n\ts_waitcnt lgkmcnt(5)\n\tv_mfma_f32_16x16x32_bf16 v[116:119], a[16:19], v[144:147], 0\n\tds_read_b128 a[168:171], v228 offset:9216\n\tv_mfma_f32_16x16x32_bf16 v[124:127], a[64:67], v[144:147], 0\n\tv_mfma_f32_16x16x32_bf16 v[112:115], a[112:115], v[144:147], 0\n\ts_waitcnt lgkmcnt(3)\n\tv_mfma_f32_16x16x32_bf16 v[116:119], a[20:23], v[148:151], v[116:119]\n\tv_mfma_f32_16x16x32_bf16 v[124:127], a[68:71], v[148:151], v[124:127]\n\ts_waitcnt lgkmcnt(1)\n\tbuffer_store_dwordx4 a[184:187], v229, s[28:31], s41 offen sc1\n\tv_mfma_f32_16x16x32_bf16 v[112:115], a[116:119], v[148:151], v[112:115]\n\ts_mov_b32 s41, 28672\n\tv_mfma_f32_16x16x32_bf16 v[116:119], a[40:43], v[152:155], v[116:119]\n\tv_mfma_f32_16x16x32_bf16 v[124:127], a[88:91], v[152:155], v[124:127]\n\tv_mfma_f32_16x16x32_bf16 v[112:115], a[136:139], v[152:155], v[112:115]\n\tv_mfma_f32_16x16x32_bf16 v[116:119], a[44:47], v[156:159], v[116:119]\n\tv_mfma_f32_16x16x32_bf16 v[124:127], a[92:95], v[156:159], v[124:127]\n\ts_waitcnt lgkmcnt(0)\n\tbuffer_store_dwordx4 a[168:171], v230, s[28:31], s41 offen sc1\n\tv_mfma_f32_16x16x32_bf16 v[112:115], a[140:143], v[156:159], v[112:115]\n\ts_mov_b32 s41, 32256\n\ts_nop 0\n\tv_mfma_f32_16x16x16_bf16 v[116:119], v[250:251], v[160:161], v[116:119]\n\tv_mfma_f32_16x16x16_bf16 v[124:127], v[252:253], v[160:161], v[124:127]\n\ts_nop 1\n\tv_mfma_f32_16x16x16_bf16 v[112:115], v[254:255], v[160:161], v[112:115]\n\ts_nop 3\n\tv_cvt_pk_bf16_f32 v120, v116, v117\n\tv_cvt_pk_bf16_f32 v121, v118, v119\n\tv_cvt_pk_bf16_f32 v122, v124, v125\n\tv_cvt_pk_bf16_f32 v123, v126, v127\n\tds_write_b128 v225, v[120:123] offset:12800\n\tv_cvt_pk_bf16_f32 v112, v112, v113\n\tv_cvt_pk_bf16_f32 v113, v114, v115\n\tds_write_b64 v226, v[112:113] offset:13056\n\ts_waitcnt lgkmcnt(0)\n\ts_barrier\n\tds_read_b128 a[172:175], v228 offset:12800\n\tds_read_b128 a[176:179], v228 offset:13824\n\ts_waitcnt lgkmcnt(1)\n\tbuffer_store_dwordx4 a[172:175], v229, s[28:31], s41 offen sc1\n\ts_mov_b32 s41, 33280\n\ts_waitcnt lgkmcnt(0)\n\tbuffer_store_dwordx4 a[176:179], v230, s[28:31], s41 offen sc1\n\tCE%=:"
    : [tid] "+v"(tid)
    : [karg] "s"(karg), [bid] "s"(bid), [ldsb] "s"(ldsb)
    : "memory", "vcc", "scc", "s8","s9","s10","s11","s12","s13","s14","s15","s16","s17","s18","s19","s20","s21","s22","s23","s24","s25","s26","s27","s28","s29","s30","s31","s36","s37","s38","s39","s40","s41","s42","s43","s44","s45","s46","s47","s48","s49","s50","s51","s52","s53","s54","s55","s56","s57","s58","s59","s60","s61","s62","s63","s64","s65","s66","s67","s68","s69","s70","s71","s72","s73","s74","s75","s76","s77","s78","s79","s80","s81","s82","s83","s84","s85","s86","s87","s88","s89","s90","s91","s92","s93","s94","s95", "v1","v2","v3","v4","v5","v6","v7","v8","v9","v10","v11","v12","v13","v14","v15","v16","v17","v18","v19","v20","v21","v22","v23","v24","v25","v26","v27","v28","v29","v30","v31","v32","v33","v34","v35","v36","v37","v38","v39","v40","v41","v42","v43","v44","v45","v46","v47","v48","v49","v50","v51","v52","v53","v54","v55","v56","v57","v58","v59","v60","v61","v62","v63","v64","v65","v66","v67","v68","v69","v70","v71","v72","v73","v74","v75","v76","v77","v78","v79","v80","v81","v82","v83","v84","v85","v86","v87","v88","v89","v90","v91","v92","v93","v94","v95","v96","v97","v98","v99","v100","v101","v102","v103","v104","v105","v106","v107","v108","v109","v110","v111","v112","v113","v114","v115","v116","v117","v118","v119","v120","v121","v122","v123","v124","v125","v126","v127","v128","v129","v130","v131","v132","v133","v134","v135","v136","v137","v138","v139","v140","v141","v142","v143","v144","v145","v146","v147","v148","v149","v150","v151","v152","v153","v154","v155","v156","v157","v158","v159","v160","v161","v162","v163","v164","v165","v166","v167","v168","v169","v170","v171","v172","v173","v174","v175","v176","v177","v178","v179","v180","v181","v182","v183","v184","v185","v186","v187","v188","v189","v190","v191","v192","v193","v194","v195","v196","v197","v198","v199","v200","v201","v202","v203","v204","v205","v206","v207","v208","v209","v210","v211","v212","v213","v214","v215","v216","v217","v218","v219","v220","v221","v222","v223","v224","v225","v226","v227","v228","v229","v230","v231","v232","v233","v234","v235","v236","v237","v238","v239","v240","v241","v242","v243","v244","v245","v246","v247","v248","v249","v250","v251","v252","v253","v254","v255", "a0","a1","a2","a3","a4","a5","a6","a7","a8","a9","a10","a11","a12","a13","a14","a15","a16","a17","a18","a19","a20","a21","a22","a23","a24","a25","a26","a27","a28","a29","a30","a31","a32","a33","a34","a35","a36","a37","a38","a39","a40","a41","a42","a43","a44","a45","a46","a47","a48","a49","a50","a51","a52","a53","a54","a55","a56","a57","a58","a59","a60","a61","a62","a63","a64","a65","a66","a67","a68","a69","a70","a71","a72","a73","a74","a75","a76","a77","a78","a79","a80","a81","a82","a83","a84","a85","a86","a87","a88","a89","a90","a91","a92","a93","a94","a95","a96","a97","a98","a99","a100","a101","a102","a103","a104","a105","a106","a107","a108","a109","a110","a111","a112","a113","a114","a115","a116","a117","a118","a119","a120","a121","a122","a123","a124","a125","a126","a127","a128","a129","a130","a131","a132","a133","a134","a135","a136","a137","a138","a139","a140","a141","a142","a143","a144","a145","a146","a147","a148","a149","a150","a151","a152","a153","a154","a155","a156","a157","a158","a159","a160","a161","a162","a163","a164","a165","a166","a167","a168","a169","a170","a171","a172","a173","a174","a175","a176","a177","a178","a179","a180","a181","a182","a183","a184","a185","a186","a187","a188","a189","a190","a191","a192","a193","a194","a195","a196","a197","a198","a199","a200","a201","a202","a203","a204","a205","a206","a207","a208","a209","a210","a211","a212","a213","a214","a215","a216","a217","a218","a219","a220","a221","a222","a223","a224","a225","a226","a227","a228","a229","a230","a231","a232","a233","a234","a235","a236","a237","a238","a239","a240","a241","a242","a243","a244","a245","a246","a247","a248","a249","a250","a251","a252","a253","a254","a255");

}

extern "C" void kernel_launch(void* const* d_in, const int* in_sizes, int n_in, void* d_out, int out_size,
                              void* d_ws, size_t ws_size, hipStream_t stream) {
  standalone__autoregressive_conv_wavenet__speech_kernel<<<250, 256, 0, stream>>>(d_in[0], d_in[1], d_in[2], d_out);
}
```
